```python
import torch

def _softmax(x, axis=-1):
    return torch.softmax(x, dim=axis)
def _gelu(x):
    return torch.nn.functional.gelu(x, approximate="tanh")
def _silu(x):
    return torch.nn.functional.silu(x)
def _sigmoid(x):
    return torch.sigmoid(x)
def _layernorm(x, axis=-1, eps=1e-5):
    m = x.mean(dim=axis, keepdim=True)
    v = x.var(dim=axis, keepdim=True, unbiased=False)
    return (x - m) / torch.sqrt(v + eps)
def _scatter_add(tgt, idx, src, axis=0):
    return tgt.clone().index_add_(axis, idx, src)
def _segment_sum(src, seg, n):
    out = torch.zeros((n,) + src.shape[1:], dtype=src.dtype)
    return out.index_add_(0, seg, src)
def _pad(x, pw):
    flat = tuple(v for pair in reversed(pw) for v in pair)
    return torch.nn.functional.pad(x, flat)

kernel_name = 'concat_heads__mha_block__spectral_fnet__prompt'

def setup_inputs():

    _w = (lambda a: (lambda t: t.to(torch.bfloat16) if t.is_floating_point() else t)(torch.as_tensor(a)))
    def _A_setup():
        B, S, D, H = 1, 2048, 512, 32
        return {
            "x": _w((lambda *s: torch.randn(*s, dtype=torch.bfloat16) * 0.2)(B, S, D)),
            "wqkv": _w((lambda *s: torch.randn(*s, dtype=torch.bfloat16) * 0.8)(D, 3 * D)),
            "wo": _w((lambda *s: torch.randn(*s, dtype=torch.bfloat16) * 0.2)(D, D)),
        }
    _A_in = _A_setup()

    def _B_setup():
        B, S, D = 1, 2048, 512
        ns = torch.arange(S)[:, None] * torch.arange(S)[None, :]
        nd = torch.arange(D)[:, None] * torch.arange(D)[None, :]
        Ws_re = torch.cos(-2.0 * torch.pi * ns / S).to(float)
        Ws_im = torch.sin(-2.0 * torch.pi * ns / S).to(float)
        Wd_re = torch.cos(-2.0 * torch.pi * nd / D).to(float)
        Wd_im = torch.sin(-2.0 * torch.pi * nd / D).to(float)
        return {
            "x": _w((lambda *s: torch.randn(*s, dtype=torch.bfloat16) * 0.05)(B, S, D)),
            "dft_seq_re": _w(Ws_re),
            "dft_seq_im": _w(Ws_im),
            "dft_hid_re": _w(Wd_re),
            "dft_hid_im": _w(Wd_im),
        }
    _B_in = _B_setup()

    return {
        "A_x": _A_in["x"],
        "A_wqkv": _A_in["wqkv"],
        "A_wo": _A_in["wo"],
        "B_x": _B_in["x"],
        "B_dft_seq_re": _B_in["dft_seq_re"],
        "B_dft_seq_im": _B_in["dft_seq_im"],
        "B_dft_hid_re": _B_in["dft_hid_re"],
        "B_dft_hid_im": _B_in["dft_hid_im"],
    }

def reference(A_x, A_wqkv, A_wo, B_x, B_dft_seq_re, B_dft_seq_im, B_dft_hid_re, B_dft_hid_im):
    def _A_ref(x, wqkv, wo):
        B, S, D = x.shape
        H = 32
        Dh = D // H
        qkv = (lambda x, *d: x.permute(*d))((x @ wqkv).reshape(B, S, 3, H, Dh), 2, 0, 3, 1, 4)
        q, k, v = qkv[0], qkv[1], qkv[2]
        scores = torch.einsum("bhqd,bhkd->bhqk", q, k) * (Dh ** -0.5)
        p = _softmax(scores, axis=-1)
        o = torch.einsum("bhqk,bhkd->bhqd", p, v)
        return (lambda x, *d: x.permute(*d))(o, 0, 2, 1, 3).reshape(B, S, D) @ wo

    def _B_ref(x, dft_seq_re, dft_seq_im, dft_hid_re, dft_hid_im):
        a = torch.einsum("bsd,ed->bse", x, dft_hid_re)
        b = torch.einsum("bsd,ed->bse", x, dft_hid_im)
        y = torch.einsum("ts,bse->bte", dft_seq_re, a) - torch.einsum("ts,bse->bte", dft_seq_im, b)
        return y

    a = _A_ref(A_x, A_wqkv, A_wo)
    b = _B_ref(B_x, B_dft_seq_re, B_dft_seq_im, B_dft_hid_re, B_dft_hid_im)
    return torch.concatenate([a, b], axis=-1)
```

```cpp
#include <hip/hip_runtime.h>
#include <hip/hip_bf16.h>
#include <stdint.h>

typedef unsigned short u16;
__device__ __forceinline__ float b2f(u16 v) { return __uint_as_float(((unsigned)v) << 16); }
__device__ __forceinline__ u16 f2b(float f) { unsigned u = __float_as_uint(f); u += 0x7fffu + ((u >> 16) & 1u); return (u16)(u >> 16); }

constexpr int S = 2048, D = 512, H = 32, DH = 16;

__global__ void gemm_nn(const u16* A, const u16* B, u16* C, int M, int N, int K, int ldc) {
  int n = blockIdx.x * blockDim.x + threadIdx.x, m = blockIdx.y;
  if (n >= N) return;
  float acc = 0;
  for (int k = 0; k < K; ++k) acc += b2f(A[(size_t)m * K + k]) * b2f(B[(size_t)k * N + n]);
  C[(size_t)m * ldc + n] = f2b(acc);
}
__global__ void gemm_nt(const u16* A, const u16* B, u16* C, int M, int N, int K, int ldc) {
  int n = blockIdx.x * blockDim.x + threadIdx.x, m = blockIdx.y;
  if (n >= N) return;
  float acc = 0;
  for (int k = 0; k < K; ++k) acc += b2f(A[(size_t)m * K + k]) * b2f(B[(size_t)n * K + k]);
  C[(size_t)m * ldc + n] = f2b(acc);
}
__global__ void attn(const u16* qkv, u16* o) {
  int q = blockIdx.x * blockDim.x + threadIdx.x, h = blockIdx.y;
  float qv[DH];
  for (int d = 0; d < DH; ++d) qv[d] = b2f(qkv[(size_t)q * 3 * D + h * DH + d]);
  float m = -INFINITY, l = 0, acc[DH];
  for (int d = 0; d < DH; ++d) acc[d] = 0;
  for (int k = 0; k < S; ++k) {
    float s = 0;
    for (int d = 0; d < DH; ++d) s += qv[d] * b2f(qkv[(size_t)k * 3 * D + D + h * DH + d]);
    s *= 0.25f;
    float mn = fmaxf(m, s), a = __expf(m - mn), p = __expf(s - mn);
    l = l * a + p;
    for (int d = 0; d < DH; ++d) acc[d] = acc[d] * a + p * b2f(qkv[(size_t)k * 3 * D + 2 * D + h * DH + d]);
    m = mn;
  }
  for (int d = 0; d < DH; ++d) o[(size_t)q * D + h * DH + d] = f2b(acc[d] / l);
}
__global__ void seqdft(const u16* Cr, const u16* Ci, const u16* a, const u16* b, u16* out) {
  int e = blockIdx.x * blockDim.x + threadIdx.x, t = blockIdx.y;
  float r = 0, i = 0;
  for (int s = 0; s < S; ++s) { r += b2f(Cr[(size_t)t * S + s]) * b2f(a[(size_t)s * D + e]); i += b2f(Ci[(size_t)t * S + s]) * b2f(b[(size_t)s * D + e]); }
  out[(size_t)t * 2 * D + D + e] = f2b(r - i);
}

extern "C" void kernel_launch(void* const* d_in, const int* in_sizes, int n_in, void* d_out, int out_size, void* d_ws, size_t ws_size, hipStream_t stream) {
  const u16* x = (const u16*)d_in[0]; const u16* wqkv = (const u16*)d_in[1]; const u16* wo = (const u16*)d_in[2];
  const u16* xb = (const u16*)d_in[3]; const u16* cr = (const u16*)d_in[4]; const u16* ci = (const u16*)d_in[5];
  const u16* dr = (const u16*)d_in[6]; const u16* di = (const u16*)d_in[7];
  u16* out = (u16*)d_out;
  u16* qkv = (u16*)d_ws; u16* o = qkv + S * 3 * D; u16* a = o + S * D; u16* b = a + S * D;
  gemm_nn<<<dim3(3 * D / 256, S), 256, 0, stream>>>(x, wqkv, qkv, S, 3 * D, D, 3 * D);
  gemm_nt<<<dim3(D / 256, S), 256, 0, stream>>>(xb, dr, a, S, D, D, D);
  gemm_nt<<<dim3(D / 256, S), 256, 0, stream>>>(xb, di, b, S, D, D, D);
  attn<<<dim3(S / 64, H), 64, 0, stream>>>(qkv, o);
  gemm_nn<<<dim3(D / 256, S), 256, 0, stream>>>(o, wo, out, S, D, D, 2 * D);
  seqdft<<<dim3(D / 256, S), 256, 0, stream>>>(cr, ci, a, b, out);
}
```

```cpp
#include <hip/hip_runtime.h>
#include <stdint.h>

typedef __bf16 bf16x8  __attribute__((ext_vector_type(8)));
typedef __bf16 bf16x16 __attribute__((ext_vector_type(16)));
typedef __bf16 bf16x2  __attribute__((ext_vector_type(2)));
typedef short  s16x4   __attribute__((ext_vector_type(4)));
typedef short  s16x8   __attribute__((ext_vector_type(8)));
typedef float  f32x2   __attribute__((ext_vector_type(2)));
typedef float  f32x8   __attribute__((ext_vector_type(8)));
typedef float  f32x16  __attribute__((ext_vector_type(16)));
typedef unsigned u32x4 __attribute__((ext_vector_type(4)));
typedef unsigned u32x2 __attribute__((ext_vector_type(2)));
#define LDSP __attribute__((address_space(3)))
#define DEV __device__ __forceinline__

constexpr int S = 2048, D = 512;
constexpr size_t WS_Q  = 0;
constexpr size_t WS_KV = 2u << 20;
constexpr size_t WS_AB = (6u << 20) + 65536;
constexpr size_t WS_O  = WS_AB + (4u << 20);
constexpr size_t WS_P  = WS_O + (2u << 20);

#define STAMP_DECL
#define STAMPR(i) do {} while (0)
#define STAMPRT(i) do {} while (0)
#define STAMP_FLUSH(kern, n) do {} while (0)
#define SB __builtin_amdgcn_sched_barrier(0)
#define MFMA(a, b, c) __builtin_amdgcn_mfma_f32_32x32x16_bf16(a, b, c, 0, 0, 0)

DEV void glds(uint32_t ldsaddr, const void* sbase, uint32_t voff) {
  asm volatile("s_mov_b32 m0, %2\n\ts_nop 0\n\tglobal_load_lds_dwordx4 %0, %1" ::"v"(voff), "s"(sbase), "s"(ldsaddr) : "memory");
}
template <int N> DEV void wait_vm() { asm volatile("s_waitcnt vmcnt(%0)" ::"n"(N) : "memory"); }
DEV void barrier_lgkm() { asm volatile("s_waitcnt lgkmcnt(0)\n\ts_barrier" ::: "memory"); }
DEV bf16x8 ldsr(uint32_t a) { return *(const LDSP bf16x8*)(size_t)a; }
DEV bf16x8 ldstr(uint32_t a0, uint32_t a1) {
  s16x4 x = __builtin_amdgcn_ds_read_tr16_b64_v4i16((LDSP s16x4*)(size_t)a0);
  s16x4 y = __builtin_amdgcn_ds_read_tr16_b64_v4i16((LDSP s16x4*)(size_t)a1);
  return __builtin_bit_cast(bf16x8, (s16x8)__builtin_shufflevector(x, y, 0, 1, 2, 3, 4, 5, 6, 7));
}
DEV unsigned pk(float a, float b) {
  f32x2 v = {a, b};
  return __builtin_bit_cast(unsigned, __builtin_convertvector(v, bf16x2));
}
DEV u32x4 pk8(const f32x16& a, int o, float sc) {
  u32x4 r;
  r[0] = pk(a[o + 0] * sc, a[o + 1] * sc); r[1] = pk(a[o + 2] * sc, a[o + 3] * sc);
  r[2] = pk(a[o + 4] * sc, a[o + 5] * sc); r[3] = pk(a[o + 6] * sc, a[o + 7] * sc);
  return r;
}
DEV u32x4 pk8n(const f32x16& a, int o) {
  u32x4 r;
  r[0] = pk(a[o + 0], a[o + 1]); r[1] = pk(a[o + 2], a[o + 3]);
  r[2] = pk(a[o + 4], a[o + 5]); r[3] = pk(a[o + 6], a[o + 7]);
  return r;
}
DEV u32x4 swap8(u32x4 v) {
  auto a = __builtin_amdgcn_permlane32_swap(v[0], v[2], false, false);
  auto b = __builtin_amdgcn_permlane32_swap(v[1], v[3], false, false);
  u32x4 r; r[0] = a[0]; r[1] = b[0]; r[2] = a[1]; r[3] = b[1];
  return r;
}

__global__ __launch_bounds__(512) void k1_proj(const char* __restrict__ xA, const char* __restrict__ wqkv, const char* __restrict__ xB,
                                               const char* __restrict__ hre, const char* __restrict__ him, char* __restrict__ ws) {
  __shared__ __attribute__((aligned(16))) char smem[163840];
  const uint32_t lds0 = (uint32_t)(size_t)(LDSP char*)smem;
  const int l = threadIdx.x & 63, w = __builtin_amdgcn_readfirstlane(threadIdx.x >> 6);
  const int h = l >> 5, r32 = l & 31;
  const int b = blockIdx.x, xcd = b & 7, idx = b >> 3;
  const int role = idx >> 4, i16 = idx & 15;
  const int mt = (xcd >> 1) * 4 + (i16 >> 2), nt = (xcd & 1) * 4 + (i16 & 3);
  const int wm = w & 3, wn = w >> 2;
  STAMP_DECL; STAMPRT(0); STAMPR(1);
  const uint32_t xsw = (uint32_t)(((l & 7) ^ ((4 * (w & 1) + (l >> 4)) & 7)) * 16);
  const uint32_t voffX = (uint32_t)(8 * w + (l >> 3)) * 1024u + xsw;
  const uint32_t rdX = (uint32_t)(32 * wm + r32) * 128u + (uint32_t)((h ^ ((r32 >> 1) & 7)) * 16);
  if (role == 0) {
    constexpr uint32_t BUF = 40960;
    const char* gx = xA + (size_t)mt * 128 * 1024;
    const char* gw = wqkv + nt * 128;
    const uint32_t voffW = (uint32_t)(8 * w + (l >> 3)) * 3072u + (uint32_t)(((l & 7) ^ (((l >> 4) & 1) << 2)) * 16);
    auto issue = [&](int p) {
      const uint32_t bb = lds0 + (p & 3) * BUF;
      glds(bb + w * 1024, gx + p * 128, voffX);
      glds(bb + (w + 8) * 1024, gx + p * 128 + 65536, voffX);
#pragma unroll
      for (int g = 0; g < 3; ++g) glds(bb + 16384 + g * 8192 + w * 1024, gw + (size_t)p * 64 * 3072 + g * 1024, voffW);
    };
    issue(0); issue(1); issue(2); issue(3);
    const int q = (l & 15) >> 2, pp = l & 3, blk = (l >> 4) & 1;
    const uint32_t rdW = 16384u + (uint32_t)(8 * h + q) * 128u + (uint32_t)(((4 * wn + 2 * blk + (pp >> 1)) ^ (4 * (q >> 1))) * 16 + 8 * (pp & 1));
    f32x16 aq, ak, av;
#pragma unroll
    for (int i = 0; i < 16; ++i) { aq[i] = 0.f; ak[i] = 0.f; av[i] = 0.f; }
#pragma unroll
    for (int p = 0; p < 8; ++p) {
      if (p == 0) wait_vm<15>(); else if (p <= 5) wait_vm<10>(); else if (p == 6) wait_vm<5>(); else wait_vm<0>();
      barrier_lgkm();
      STAMPR(2 + p);
      if (p >= 1 && p + 3 < 8) issue(p + 3);
      const uint32_t bb = lds0 + (p & 3) * BUF;
#pragma unroll
      for (int ks = 0; ks < 4; ++ks) {
        bf16x8 x = ldsr(bb + (rdX ^ (ks << 5)));
        bf16x8 wq = ldstr(bb + rdW + ks * 2048, bb + rdW + ks * 2048 + 512);
        bf16x8 wk = ldstr(bb + rdW + 8192 + ks * 2048, bb + rdW + 8192 + ks * 2048 + 512);
        bf16x8 wv = ldstr(bb + rdW + 16384 + ks * 2048, bb + rdW + 16384 + ks * 2048 + 512);
        aq = MFMA(wq, x, aq);
        ak = MFMA(wk, x, ak);
        av = MFMA(x, wv, av);
      }
    }
    const int tile = mt * 4 + wm;
    const int head0 = nt * 4 + wn * 2;
    constexpr float QS = 0.25f * 1.4426950408889634f;
    char* qf = ws + WS_Q;
    char* kv = ws + WS_KV;
    const int pr = (r32 & 0x13) | ((r32 & 4) << 1) | ((r32 & 8) >> 1);
#pragma unroll
    for (int hh = 0; hh < 2; ++hh) {
      *(u32x4*)(qf + ((size_t)(head0 + hh) * 64 + tile) * 1024 + l * 16) = pk8(aq, 8 * hh, QS);
      *(u32x4*)(kv + ((size_t)(head0 + hh) * 64 + tile) * 2048 + (h * 32 + pr) * 16) = pk8n(ak, 8 * hh);
    }
    char* vb = kv + ((size_t)(head0 + (r32 >> 4)) * 64 + tile) * 2048 + 1024 + h * 256 + (r32 & 15) * 16;
    STAMPR(10);
    *(u32x4*)(vb) = pk8n(av, 0);
    *(u32x4*)(vb + 512) = pk8n(av, 8);
    STAMPR(11); STAMPRT(12); STAMP_FLUSH(0, 13);
  } else {
    constexpr uint32_t BUF = 32768;
    const char* gx = xB + (size_t)mt * 128 * 1024;
    const char* gw = (nt < 4 ? hre : him) + (size_t)(nt & 3) * 128 * 1024;
    auto issue = [&](int p) {
      const uint32_t bb = lds0 + (p & 3) * BUF;
      glds(bb + w * 1024, gx + p * 128, voffX);
      glds(bb + (w + 8) * 1024, gx + p * 128 + 65536, voffX);
      glds(bb + 16384 + w * 1024, gw + p * 128, voffX);
      glds(bb + 16384 + (w + 8) * 1024, gw + p * 128 + 65536, voffX);
    };
    issue(0); issue(1); issue(2); issue(3);
    const uint32_t rdE = 16384u + (uint32_t)(64 * wn + r32) * 128u + (uint32_t)((h ^ ((r32 >> 1) & 7)) * 16);
    f32x16 a0, a1;
#pragma unroll
    for (int i = 0; i < 16; ++i) { a0[i] = 0.f; a1[i] = 0.f; }
#pragma unroll
    for (int p = 0; p < 8; ++p) {
      if (p == 0) wait_vm<12>(); else if (p <= 5) wait_vm<8>(); else if (p == 6) wait_vm<4>(); else wait_vm<0>();
      barrier_lgkm();
      STAMPR(2 + p);
      if (p >= 1 && p + 3 < 8) issue(p + 3);
      const uint32_t bb = lds0 + (p & 3) * BUF;
#pragma unroll
      for (int ks = 0; ks < 4; ++ks) {
        bf16x8 x = ldsr(bb + (rdX ^ (ks << 5)));
        bf16x8 e0 = ldsr(bb + (rdE ^ (ks << 5)));
        bf16x8 e1 = ldsr(bb + (rdE ^ (ks << 5)) + 4096);
        a0 = MFMA(x, e0, a0);
        a1 = MFMA(x, e1, a1);
      }
    }
    const float sg = nt < 4 ? 1.f : -1.f;
    char* ab = ws + WS_AB + (size_t)nt * (512u << 10) + (size_t)(mt * 8 + wm * 2) * 4096 + (wn * 2) * 1024 + l * 16;
    *(u32x4*)(ab) = swap8(pk8(a0, 0, sg));
    *(u32x4*)(ab + 4096) = swap8(pk8(a0, 8, sg));
    *(u32x4*)(ab + 1024) = swap8(pk8(a1, 0, sg));
    *(u32x4*)(ab + 4096 + 1024) = swap8(pk8(a1, 8, sg));
    STAMPR(10); STAMPR(11); STAMPRT(12); STAMP_FLUSH(0, 13);
  }
}

constexpr uint32_t GBUF = 32768, KV0 = 131072, SLOT = 2080, KVBUF = 4 * SLOT;
#define SMF(a, b, c) __builtin_amdgcn_smfmac_f32_32x32x32_bf16(a, b, c, sidx, 0, 0)
#define LO8(x) __builtin_bit_cast(bf16x16, (f32x8)__builtin_shufflevector(x, x, 0, 1, 2, 3, 4, 5, 6, 7))
#define HI8(x) __builtin_bit_cast(bf16x16, (f32x8)__builtin_shufflevector(x, x, 8, 9, 10, 11, 12, 13, 14, 15))
#define PIN(x) do { asm volatile("" ::"v"(x)); SB; } while (0)
#define EXPR(s, a, b) do { _Pragma("unroll") for (int i_ = a; i_ < b; ++i_) s[i_] = __builtin_amdgcn_exp2f(s[i_]); SB; } while (0)

struct AttnState {
  f32x16 sA, sB, oA, oB, nA, nB;
  bf16x8 qA, qB, kf, v0, v1;
};

template <int BUFI, int U>
DEV void attn_step(AttnState& a, uint32_t aK, uint32_t aV0, uint32_t aV1, int sidx) {
  constexpr uint32_t OFF = BUFI * KVBUF + U * SLOT;
  EXPR(a.sA, 0, 2);
  a.oB = SMF(a.v1, HI8(a.sB), a.oB); PIN(a.oB);
  a.v1 = ldsr(aV1 + OFF); SB;
  EXPR(a.sA, 2, 7);
  a.sB = MFMA(a.kf, a.qB, a.nB); PIN(a.sB);
  a.kf = ldsr(aK + OFF); SB;
  EXPR(a.sA, 7, 12);
  a.oA = SMF(a.v0, LO8(a.sA), a.oA); PIN(a.oA);
  EXPR(a.sA, 12, 16);
  EXPR(a.sB, 0, 2);
  a.oA = SMF(a.v1, HI8(a.sA), a.oA); PIN(a.oA);
  EXPR(a.sB, 2, 7);
  a.sA = MFMA(a.kf, a.qA, a.nA); PIN(a.sA);
  EXPR(a.sB, 7, 12);
  a.oB = SMF(a.v0, LO8(a.sB), a.oB); PIN(a.oB);
  a.v0 = ldsr(aV0 + OFF); SB;
  EXPR(a.sB, 12, 16);
}
template <int BUFI>
DEV void attn_stage(AttnState& a, uint32_t aK, uint32_t aV0, uint32_t aV1, int sidx) {
  barrier_lgkm(); SB;
  attn_step<BUFI, 0>(a, aK, aV0, aV1, sidx);
  attn_step<BUFI, 1>(a, aK, aV0, aV1, sidx);
}

DEV float max16(const f32x16& s) {
  float m = fmaxf(fmaxf(fmaxf(s[0], s[1]), fmaxf(s[2], s[3])), fmaxf(fmaxf(s[4], s[5]), fmaxf(s[6], s[7])));
  float n = fmaxf(fmaxf(fmaxf(s[8], s[9]), fmaxf(s[10], s[11])), fmaxf(fmaxf(s[12], s[13]), fmaxf(s[14], s[15])));
  return fmaxf(m, n);
}
DEV float xhalf_max(float m) {
  auto t = __builtin_amdgcn_permlane32_swap(__float_as_uint(m), __float_as_uint(m), false, false);
  return fmaxf(__uint_as_float(t[0]), __uint_as_float(t[1]));
}
DEV f32x16 splat16(float v) {
  f32x16 r;
#pragma unroll
  for (int i = 0; i < 16; ++i) r[i] = v;
  return r;
}
DEV bf16x8 ones8() { u32x4 o = {0x3F803F80u, 0x3F803F80u, 0x3F803F80u, 0x3F803F80u}; return __builtin_bit_cast(bf16x8, o); }

DEV f32x16 attn_exact(const char* kvh, bf16x8 q, int l, int sidx) {
  const int h = l >> 5, r32 = l & 31;
  float m = -INFINITY;
  const f32x16 z = splat16(0.f);
#pragma unroll 1
  for (int t = 0; t < 64; ++t) {
    bf16x8 k = *(const bf16x8*)(kvh + t * 2048 + l * 16);
    f32x16 s = MFMA(k, q, z);
    m = fmaxf(m, max16(s));
  }
  m = xhalf_max(m);
  const f32x16 nm = splat16(-m);
  f32x16 o = z;
#pragma unroll 1
  for (int t = 0; t < 64; ++t) {
    bf16x8 k = *(const bf16x8*)(kvh + t * 2048 + l * 16);
    bf16x8 v0 = *(const bf16x8*)(kvh + t * 2048 + 1024 + h * 256 + (r32 & 15) * 16);
    bf16x8 v1 = *(const bf16x8*)(kvh + t * 2048 + 1536 + h * 256 + (r32 & 15) * 16);
    if (r32 >= 16) { v0 = ones8(); v1 = ones8(); }
    f32x16 s = MFMA(k, q, nm);
#pragma unroll
    for (int i = 0; i < 16; ++i) s[i] = __builtin_amdgcn_exp2f(s[i]);
    o = SMF(v0, LO8(s), o);
    o = SMF(v1, HI8(s), o);
  }
  return o;
}

__global__ __launch_bounds__(768) void k2_main(const char* __restrict__ cre, const char* __restrict__ cim, char* __restrict__ ws) {
  __shared__ __attribute__((aligned(16))) char smem[KV0 + 3 * KVBUF];
  const uint32_t lds0 = (uint32_t)(size_t)(LDSP char*)smem;
  const int l = threadIdx.x & 63, w = __builtin_amdgcn_readfirstlane(threadIdx.x >> 6);
  const int h = l >> 5, r32 = l & 31;
  const int b = blockIdx.x, xcd = b & 7, idx = b >> 3;
  const int head = xcd * 4 + (idx >> 3), qblk = idx & 7;
  const char* kvh = ws + WS_KV + (size_t)head * 131072;
  STAMP_DECL; STAMPRT(0); STAMPR(1);
  if (w < 8) {
    const int qg = w & 3, kh = w >> 2;
    int sidx = 0xDDDD;
    asm volatile("" : "+v"(sidx));
    if (threadIdx.x < 12) {
      u32x4 o = {0x3F803F80u, 0x3F803F80u, 0x3F803F80u, 0x3F803F80u};
      *(LDSP u32x4*)(size_t)(lds0 + KV0 + threadIdx.x * SLOT + 2048) = o;
    }
    AttnState a;
    const char* qf = ws + WS_Q + ((size_t)head * 64 + qblk * 8 + qg * 2) * 1024 + l * 16;
    a.qA = *(const bf16x8*)qf;
    a.qB = *(const bf16x8*)(qf + 1024);
    const bf16x8 k0 = *(const bf16x8*)(kvh + l * 16);
    a.kf = *(const bf16x8*)(kvh + kh * 65536 + l * 16);
    a.v0 = *(const bf16x8*)(kvh + kh * 65536 + 1024 + h * 256 + (r32 & 15) * 16);
    if (r32 >= 16) a.v0 = ones8();
    {
      const f32x16 z = splat16(0.f);
      f32x16 t0 = MFMA(k0, a.qA, z), t1 = MFMA(k0, a.qB, z);
      a.nA = splat16(-(xhalf_max(max16(t0)) + 64.f));
      a.nB = splat16(-(xhalf_max(max16(t1)) + 64.f));
      a.oA = z; a.oB = z; a.sB = z;
      a.v1 = __builtin_bit_cast(bf16x8, (u32x4){0u, 0u, 0u, 0u});
      a.sA = MFMA(a.kf, a.qA, a.nA);
    }
    const uint32_t sb = lds0 + KV0 + kh * 2 * SLOT;
    const bool onesl = (r32 >= 16 && r32 < 20) || r32 >= 28;
    const uint32_t vrow = h * 256 + (r32 < 16 ? r32 * 16 : 0);
    const uint32_t aK = sb + l * 16;
    const uint32_t aV0 = sb + (onesl ? 2048u : 1024u + vrow);
    const uint32_t aV1 = sb + (onesl ? 2048u : 1536u + vrow);
    STAMPR(2);
#pragma unroll 1
    for (int it = 0; it < 5; ++it) {
      attn_stage<0>(a, aK, aV0, aV1, sidx);
      attn_stage<1>(a, aK, aV0, aV1, sidx);
      attn_stage<2>(a, aK, aV0, aV1, sidx);
    }
    attn_stage<0>(a, aK, aV0, aV1, sidx);
    a.oB = SMF(a.v1, HI8(a.sB), a.oB);
    STAMPR(3);
    const int lr = h ? 12 : 8;
    float lA = h ? a.oA[12] : a.oA[8], lB = h ? a.oB[12] : a.oB[8];
    (void)lr;
    const uint32_t cb = lds0 + qg * 4608 + l * 4;
    if (kh == 1) {
#pragma unroll
      for (int i = 0; i < 8; ++i) {
        *(LDSP float*)(size_t)(cb + i * 256) = a.oA[i];
        *(LDSP float*)(size_t)(cb + (9 + i) * 256) = a.oB[i];
      }
      *(LDSP float*)(size_t)(cb + 8 * 256) = lA;
      *(LDSP float*)(size_t)(cb + 17 * 256) = lB;
    }
    barrier_lgkm();
    if (kh == 0) {
      float oa[8], ob[8];
#pragma unroll
      for (int i = 0; i < 8; ++i) {
        oa[i] = a.oA[i] + *(const LDSP float*)(size_t)(cb + i * 256);
        ob[i] = a.oB[i] + *(const LDSP float*)(size_t)(cb + (9 + i) * 256);
      }
      lA += *(const LDSP float*)(size_t)(cb + 8 * 256);
      lB += *(const LDSP float*)(size_t)(cb + 17 * 256);
      bool okA = lA >= 0x1p-100f && lA <= 0x1p120f, okB = lB >= 0x1p-100f && lB <= 0x1p120f;
#pragma unroll
      for (int i = 0; i < 8; ++i) { okA = okA && (fabsf(oa[i]) <= 0x1p126f); okB = okB && (fabsf(ob[i]) <= 0x1p126f); }
      if (__builtin_amdgcn_ballot_w64(!okA) != 0) {
        f32x16 o = attn_exact(kvh, a.qA, l, sidx);
#pragma unroll
        for (int i = 0; i < 8; ++i) oa[i] = o[i];
        lA = h ? o[12] : o[8];
      }
      if (__builtin_amdgcn_ballot_w64(!okB) != 0) {
        f32x16 o = attn_exact(kvh, a.qB, l, sidx);
#pragma unroll
        for (int i = 0; i < 8; ++i) ob[i] = o[i];
        lB = h ? o[12] : o[8];
      }
      const float rA = 1.0f / lA, rB = 1.0f / lB;
      u32x4 pa, pb;
#pragma unroll
      for (int i = 0; i < 4; ++i) { pa[i] = pk(oa[2 * i] * rA, oa[2 * i + 1] * rA); pb[i] = pk(ob[2 * i] * rB, ob[2 * i + 1] * rB); }
      char* of = ws + WS_O + ((size_t)(qblk * 8 + qg * 2) * 32 + head) * 1024 + l * 16;
      *(u32x4*)of = pa;
      *(u32x4*)(of + 32768) = pb;
    }
    STAMPR(4); STAMPRT(5); STAMP_FLUSH(1, 6);
  } else {
    const int g = w - 8, ge = g & 1, gt = g >> 1;
    const int q = xcd >> 1, mtile = (xcd & 1) * 8 + (idx >> 2), ntile = idx & 3;
    const char* gbase; uint32_t pstride, qstride, vo0, vo1;
    if (g < 2) {
      gbase = (q < 2 ? cre : cim) + (size_t)(mtile * 128 + g * 64) * 4096 + (q & 1) * 2048;
      pstride = 128; qstride = 65536;
      vo0 = (uint32_t)(l >> 3) * 4096u + (uint32_t)(((l & 7) ^ (l >> 4)) * 16);
      vo1 = (uint32_t)(8 + (l >> 3)) * 4096u + (uint32_t)(((l & 7) ^ (4 + (l >> 4))) * 16);
    } else {
      gbase = ws + WS_AB + (size_t)((q >> 1) * 4 + ntile) * (512u << 10) + (size_t)(q & 1) * 64 * 4096 + (g - 2) * 8192;
      pstride = 16384; qstride = 2048;
      vo0 = l * 16; vo1 = l * 16 + 1024;
    }
    const char* kvs = kvh + (size_t)((g >> 1) * 32 + (g & 1)) * 2048;
    const uint32_t voK = l * 16 + 2048, voV = l * 16 + 1024 + (l < 32 ? 2048 : 0);
    auto issueG = [&](int p) {
      const char* sb = gbase + (size_t)p * pstride;
      const uint32_t d = lds0 + (p & 3) * GBUF + g * 8192;
#pragma unroll
      for (int i = 0; i < 8; ++i) glds(d + i * 1024, sb + (size_t)(i >> 1) * qstride, (i & 1) ? vo1 : vo0);
    };
    auto issueKV = [&](int p) {
      const uint32_t d = lds0 + KV0 + (p % 3) * KVBUF + g * SLOT;
      glds(d, kvs + p * 4096, voK);
      glds(d + 1024, kvs + p * 4096, voV);
    };
    issueG(0); issueKV(0); issueG(1); issueKV(1); issueG(2);
    uint32_t rdC[2][4], rdA[2];
#pragma unroll
    for (int ks = 0; ks < 4; ++ks) {
      rdC[0][ks] = lds0 + (((uint32_t)(64 * gt + r32) * 128u + (uint32_t)((h ^ ((r32 >> 1) & 7)) * 16)) ^ (ks << 5));
      rdC[1][ks] = rdC[0][ks] + 65536;
    }
    rdA[0] = lds0 + 16384 + ge * 2048 + l * 16; rdA[1] = rdA[0] + 65536;
    f32x16 c00 = splat16(0.f), c01 = c00, c10 = c00, c11 = c00;
#pragma unroll
    for (int p = 0; p < 16; ++p) {
      if (p == 0) STAMPR(2);
      if (p <= 13) wait_vm<18>(); else if (p == 14) wait_vm<10>(); else wait_vm<0>();
      if (p < 8) STAMPR(22 + p);
      barrier_lgkm();
      STAMPR(3 + p);
      if (p + 2 < 16) issueKV(p + 2);
      if (p + 3 < 16) issueG(p + 3);
      const int hi = (p >> 1) & 1; const uint32_t bo = (p & 1) * GBUF;
#pragma unroll
      for (int ks = 0; ks < 4; ++ks) {
        bf16x8 e0 = ldsr(rdA[hi] + bo + ks * 4096), e1 = ldsr(rdA[hi] + bo + ks * 4096 + 1024);
        bf16x8 t0 = ldsr(rdC[hi][ks] + bo), t1 = ldsr(rdC[hi][ks] + bo + 4096);
        c00 = MFMA(e0, t0, c00); c01 = MFMA(e0, t1, c01);
        c10 = MFMA(e1, t0, c10); c11 = MFMA(e1, t1, c11);
      }
    }
    barrier_lgkm();
    char* pp = ws + WS_P + (size_t)q * (2u << 20) + ((size_t)(mtile * 4 + 2 * gt) * 16 + ntile * 4 + 2 * ge) * 2048 + l * 16;
    *(u32x4*)(pp) = pk8n(c00, 0);                 *(u32x4*)(pp + 1024) = pk8n(c00, 8);
    *(u32x4*)(pp + 32768) = pk8n(c01, 0);         *(u32x4*)(pp + 32768 + 1024) = pk8n(c01, 8);
    *(u32x4*)(pp + 2048) = pk8n(c10, 0);          *(u32x4*)(pp + 2048 + 1024) = pk8n(c10, 8);
    *(u32x4*)(pp + 32768 + 2048) = pk8n(c11, 0);  *(u32x4*)(pp + 32768 + 2048 + 1024) = pk8n(c11, 8);
    STAMPR(19); STAMPR(20); STAMPRT(21); STAMP_FLUSH(1, 30);
  }
}

__global__ __launch_bounds__(256) void k3_out(const char* __restrict__ wo, const char* __restrict__ ws, char* __restrict__ out) {
  __shared__ __attribute__((aligned(16))) char smem[131072];
  const uint32_t lds0 = (uint32_t)(size_t)(LDSP char*)smem;
  const int l = threadIdx.x & 63, w = __builtin_amdgcn_readfirstlane(threadIdx.x >> 6);
  const int h = l >> 5, r32 = l & 31;
  const int b = blockIdx.x, xcd = b & 7, idx = b >> 3;
  const int st = xcd * 4 + (idx >> 3), nt = idx & 7;
  const int w0 = w & 1, w1 = w >> 1;
  STAMP_DECL; STAMPRT(0); STAMPR(1);
  const char* of = ws + WS_O + ((size_t)(st * 2 + w0) * 32 + 4 * w1) * 1024;
  const char* wg = wo + (size_t)(32 * w) * 1024 + nt * 128;
  const uint32_t voO = l * 16;
  const uint32_t voW = (uint32_t)(l >> 3) * 1024u + (uint32_t)(((l & 7) ^ (((l >> 4) & 1) << 2)) * 16);
#pragma unroll
  for (int c = 0; c < 4; ++c) {
#pragma unroll
    for (int i = 0; i < 4; ++i) glds(lds0 + (w0 * 32 + 8 * c + 4 * w1 + i) * 1024, of + (8 * c + i) * 1024, voO);
#pragma unroll
    for (int i = 0; i < 4; ++i) glds(lds0 + 65536 + (128 * c + 32 * w + 8 * i) * 128, wg + (size_t)(128 * c + 8 * i) * 1024, voW);
  }
  const char* pp = ws + WS_P + ((size_t)(st * 2 + w0) * 16 + nt * 2 + w1) * 2048 + l * 16;
  u32x4 pv[4][2];
#pragma unroll
  for (int q = 0; q < 4; ++q) {
    pv[q][0] = *(const u32x4*)(pp + (size_t)q * (2u << 20));
    pv[q][1] = *(const u32x4*)(pp + (size_t)q * (2u << 20) + 1024);
  }
  const int q4 = (l & 15) >> 2, p4 = l & 3, blk = (l >> 4) & 1;
  const uint32_t rdW = lds0 + 65536u + (uint32_t)(4 * h + q4) * 128u + (uint32_t)(((4 * w1 + 2 * blk + (p4 >> 1)) ^ (4 * (q4 >> 1))) * 16 + 8 * (p4 & 1));
  const uint32_t rdO = lds0 + w0 * 32768 + l * 16;
  f32x16 acc = splat16(0.f);
#pragma unroll
  for (int c = 0; c < 4; ++c) {
    if (c == 0) STAMPR(2);
    if (c == 0) wait_vm<32>(); else if (c == 1) wait_vm<24>(); else if (c == 2) wait_vm<16>(); else wait_vm<8>();
    barrier_lgkm();
    STAMPR(3 + c);
#pragma unroll
    for (int i = 0; i < 8; ++i) {
      const int hd = 8 * c + i;
      bf16x8 o = ldsr(rdO + hd * 1024);
      bf16x8 wf = ldstr(rdW + hd * 2048, rdW + hd * 2048 + 1024);
      acc = MFMA(wf, o, acc);
    }
  }
  char* orow = out + (size_t)(st * 64 + w0 * 32 + r32) * 2048 + (nt * 64 + w1 * 32 + 8 * h) * 2;
  *(u32x4*)(orow) = swap8(pk8n(acc, 0));
  *(u32x4*)(orow + 32) = swap8(pk8n(acc, 8));
#pragma unroll
  for (int r = 0; r < 2; ++r) {
    u32x4 o;
#pragma unroll
    for (int i = 0; i < 4; ++i) {
      float lo = 0.f, hi = 0.f;
#pragma unroll
      for (int q = 0; q < 4; ++q) { lo += __uint_as_float(pv[q][r][i] << 16); hi += __uint_as_float(pv[q][r][i] & 0xffff0000u); }
      o[i] = pk(lo, hi);
    }
    *(u32x4*)(orow + 1024 + 32 * r) = swap8(o);
  }
  STAMPR(7); STAMPRT(8); STAMP_FLUSH(2, 9);
}

extern "C" void kernel_launch(void* const* d_in, const int* in_sizes, int n_in, void* d_out, int out_size, void* d_ws, size_t ws_size,
                              hipStream_t stream) {
  (void)in_sizes; (void)n_in; (void)out_size; (void)ws_size;
  k1_proj<<<256, 512, 0, stream>>>((const char*)d_in[0], (const char*)d_in[1], (const char*)d_in[3], (const char*)d_in[6],
                                   (const char*)d_in[7], (char*)d_ws);
  k2_main<<<256, 768, 0, stream>>>((const char*)d_in[4], (const char*)d_in[5], (char*)d_ws);
  k3_out<<<256, 256, 0, stream>>>((const char*)d_in[2], (const char*)d_ws, (char*)d_out);
}
```

```cpp
#include <hip/hip_runtime.h>
#include <stdint.h>
#include <stdio.h>
#define STAMP 1

typedef __bf16 bf16x8  __attribute__((ext_vector_type(8)));
typedef __bf16 bf16x16 __attribute__((ext_vector_type(16)));
typedef __bf16 bf16x2  __attribute__((ext_vector_type(2)));
typedef short  s16x4   __attribute__((ext_vector_type(4)));
typedef short  s16x8   __attribute__((ext_vector_type(8)));
typedef float  f32x2   __attribute__((ext_vector_type(2)));
typedef float  f32x8   __attribute__((ext_vector_type(8)));
typedef float  f32x16  __attribute__((ext_vector_type(16)));
typedef unsigned u32x4 __attribute__((ext_vector_type(4)));
typedef unsigned u32x2 __attribute__((ext_vector_type(2)));
#define LDSP __attribute__((address_space(3)))
#define DEV __device__ __forceinline__

constexpr int S = 2048, D = 512;
constexpr size_t WS_Q  = 0;
constexpr size_t WS_KV = 2u << 20;
constexpr size_t WS_AB = (6u << 20) + 65536;
constexpr size_t WS_O  = WS_AB + (4u << 20);
constexpr size_t WS_P  = WS_O + (2u << 20);

constexpr size_t WS_DBG = 64u << 20;
#define STAMP_DECL unsigned long long st_[32]
#define STAMPR(i) do { SB; st_[i] = __builtin_amdgcn_s_memtime(); SB; } while (0)
#define STAMPRT(i) do { st_[i] = __builtin_amdgcn_s_memrealtime(); } while (0)
#define STAMP_FLUSH(kern, n) do { if ((threadIdx.x & 63) == 0) { unsigned long long* d_ = (unsigned long long*)((char*)ws + WS_DBG) + ((((size_t)(kern) * 256 + blockIdx.x) * 16 + (threadIdx.x >> 6)) * 32); _Pragma("unroll") for (int i_ = 0; i_ < n; ++i_) d_[i_] = st_[i_]; } } while (0)
#define SB __builtin_amdgcn_sched_barrier(0)
#define MFMA(a, b, c) __builtin_amdgcn_mfma_f32_32x32x16_bf16(a, b, c, 0, 0, 0)

DEV void glds(uint32_t ldsaddr, const void* sbase, uint32_t voff) {
  asm volatile("s_mov_b32 m0, %2\n\ts_nop 0\n\tglobal_load_lds_dwordx4 %0, %1" ::"v"(voff), "s"(sbase), "s"(ldsaddr) : "memory");
}
template <int N> DEV void wait_vm() { asm volatile("s_waitcnt vmcnt(%0)" ::"n"(N) : "memory"); }
DEV void barrier_lgkm() { asm volatile("s_waitcnt lgkmcnt(0)\n\ts_barrier" ::: "memory"); }
DEV bf16x8 ldsr(uint32_t a) { return *(const LDSP bf16x8*)(size_t)a; }
DEV bf16x8 ldstr(uint32_t a0, uint32_t a1) {
  s16x4 x = __builtin_amdgcn_ds_read_tr16_b64_v4i16((LDSP s16x4*)(size_t)a0);
  s16x4 y = __builtin_amdgcn_ds_read_tr16_b64_v4i16((LDSP s16x4*)(size_t)a1);
  return __builtin_bit_cast(bf16x8, (s16x8)__builtin_shufflevector(x, y, 0, 1, 2, 3, 4, 5, 6, 7));
}
DEV unsigned pk(float a, float b) {
  f32x2 v = {a, b};
  return __builtin_bit_cast(unsigned, __builtin_convertvector(v, bf16x2));
}
DEV u32x4 pk8(const f32x16& a, int o, float sc) {
  u32x4 r;
  r[0] = pk(a[o + 0] * sc, a[o + 1] * sc); r[1] = pk(a[o + 2] * sc, a[o + 3] * sc);
  r[2] = pk(a[o + 4] * sc, a[o + 5] * sc); r[3] = pk(a[o + 6] * sc, a[o + 7] * sc);
  return r;
}
DEV u32x4 pk8n(const f32x16& a, int o) {
  u32x4 r;
  r[0] = pk(a[o + 0], a[o + 1]); r[1] = pk(a[o + 2], a[o + 3]);
  r[2] = pk(a[o + 4], a[o + 5]); r[3] = pk(a[o + 6], a[o + 7]);
  return r;
}
DEV u32x4 swap8(u32x4 v) {
  auto a = __builtin_amdgcn_permlane32_swap(v[0], v[2], false, false);
  auto b = __builtin_amdgcn_permlane32_swap(v[1], v[3], false, false);
  u32x4 r; r[0] = a[0]; r[1] = b[0]; r[2] = a[1]; r[3] = b[1];
  return r;
}

__global__ __launch_bounds__(512) void k1_proj(const char* __restrict__ xA, const char* __restrict__ wqkv, const char* __restrict__ xB,
                                               const char* __restrict__ hre, const char* __restrict__ him, char* __restrict__ ws) {
  __shared__ __attribute__((aligned(16))) char smem[163840];
  const uint32_t lds0 = (uint32_t)(size_t)(LDSP char*)smem;
  const int l = threadIdx.x & 63, w = __builtin_amdgcn_readfirstlane(threadIdx.x >> 6);
  const int h = l >> 5, r32 = l & 31;
  const int b = blockIdx.x, xcd = b & 7, idx = b >> 3;
  const int role = idx >> 4, i16 = idx & 15;
  const int mt = (xcd >> 1) * 4 + (i16 >> 2), nt = (xcd & 1) * 4 + (i16 & 3);
  const int wm = w & 3, wn = w >> 2;
  STAMP_DECL; STAMPRT(0); STAMPR(1);
  const uint32_t xsw = (uint32_t)(((l & 7) ^ ((4 * (w & 1) + (l >> 4)) & 7)) * 16);
  const uint32_t voffX = (uint32_t)(8 * w + (l >> 3)) * 1024u + xsw;
  const uint32_t rdX = (uint32_t)(32 * wm + r32) * 128u + (uint32_t)((h ^ ((r32 >> 1) & 7)) * 16);
  if (role == 0) {
    constexpr uint32_t BUF = 40960;
    const char* gx = xA + (size_t)mt * 128 * 1024;
    const char* gw = wqkv + nt * 128;
    const uint32_t voffW = (uint32_t)(8 * w + (l >> 3)) * 3072u + (uint32_t)(((l & 7) ^ (((l >> 4) & 1) << 2)) * 16);
    auto issue = [&](int p) {
      const uint32_t bb = lds0 + (p & 3) * BUF;
      glds(bb + w * 1024, gx + p * 128, voffX);
      glds(bb + (w + 8) * 1024, gx + p * 128 + 65536, voffX);
#pragma unroll
      for (int g = 0; g < 3; ++g) glds(bb + 16384 + g * 8192 + w * 1024, gw + (size_t)p * 64 * 3072 + g * 1024, voffW);
    };
    issue(0); issue(1); issue(2); issue(3);
    const int q = (l & 15) >> 2, pp = l & 3, blk = (l >> 4) & 1;
    const uint32_t rdW = 16384u + (uint32_t)(8 * h + q) * 128u + (uint32_t)(((4 * wn + 2 * blk + (pp >> 1)) ^ (4 * (q >> 1))) * 16 + 8 * (pp & 1));
    f32x16 aq, ak, av;
#pragma unroll
    for (int i = 0; i < 16; ++i) { aq[i] = 0.f; ak[i] = 0.f; av[i] = 0.f; }
#pragma unroll
    for (int p = 0; p < 8; ++p) {
      if (p == 0) wait_vm<15>(); else if (p <= 5) wait_vm<10>(); else if (p == 6) wait_vm<5>(); else wait_vm<0>();
      barrier_lgkm();
      STAMPR(2 + p);
      if (p >= 1 && p + 3 < 8) issue(p + 3);
      const uint32_t bb = lds0 + (p & 3) * BUF;
#pragma unroll
      for (int ks = 0; ks < 4; ++ks) {
        bf16x8 x = ldsr(bb + (rdX ^ (ks << 5)));
        bf16x8 wq = ldstr(bb + rdW + ks * 2048, bb + rdW + ks * 2048 + 512);
        bf16x8 wk = ldstr(bb + rdW + 8192 + ks * 2048, bb + rdW + 8192 + ks * 2048 + 512);
        bf16x8 wv = ldstr(bb + rdW + 16384 + ks * 2048, bb + rdW + 16384 + ks * 2048 + 512);
        aq = MFMA(wq, x, aq);
        ak = MFMA(wk, x, ak);
        av = MFMA(x, wv, av);
      }
    }
    const int tile = mt * 4 + wm;
    const int head0 = nt * 4 + wn * 2;
    constexpr float QS = 0.25f * 1.4426950408889634f;
    char* qf = ws + WS_Q;
    char* kv = ws + WS_KV;
    const int pr = (r32 & 0x13) | ((r32 & 4) << 1) | ((r32 & 8) >> 1);
#pragma unroll
    for (int hh = 0; hh < 2; ++hh) {
      *(u32x4*)(qf + ((size_t)(head0 + hh) * 64 + tile) * 1024 + l * 16) = pk8(aq, 8 * hh, QS);
      *(u32x4*)(kv + ((size_t)(head0 + hh) * 64 + tile) * 2048 + (h * 32 + pr) * 16) = pk8n(ak, 8 * hh);
    }
    char* vb = kv + ((size_t)(head0 + (r32 >> 4)) * 64 + tile) * 2048 + 1024 + h * 256 + (r32 & 15) * 16;
    STAMPR(10);
    *(u32x4*)(vb) = pk8n(av, 0);
    *(u32x4*)(vb + 512) = pk8n(av, 8);
    STAMPR(11); STAMPRT(12); STAMP_FLUSH(0, 13);
  } else {
    constexpr uint32_t BUF = 32768;
    const char* gx = xB + (size_t)mt * 128 * 1024;
    const char* gw = (nt < 4 ? hre : him) + (size_t)(nt & 3) * 128 * 1024;
    auto issue = [&](int p) {
      const uint32_t bb = lds0 + (p & 3) * BUF;
      glds(bb + w * 1024, gx + p * 128, voffX);
      glds(bb + (w + 8) * 1024, gx + p * 128 + 65536, voffX);
      glds(bb + 16384 + w * 1024, gw + p * 128, voffX);
      glds(bb + 16384 + (w + 8) * 1024, gw + p * 128 + 65536, voffX);
    };
    issue(0); issue(1); issue(2); issue(3);
    const uint32_t rdE = 16384u + (uint32_t)(64 * wn + r32) * 128u + (uint32_t)((h ^ ((r32 >> 1) & 7)) * 16);
    f32x16 a0, a1;
#pragma unroll
    for (int i = 0; i < 16; ++i) { a0[i] = 0.f; a1[i] = 0.f; }
#pragma unroll
    for (int p = 0; p < 8; ++p) {
      if (p == 0) wait_vm<12>(); else if (p <= 5) wait_vm<8>(); else if (p == 6) wait_vm<4>(); else wait_vm<0>();
      barrier_lgkm();
      STAMPR(2 + p);
      if (p >= 1 && p + 3 < 8) issue(p + 3);
      const uint32_t bb = lds0 + (p & 3) * BUF;
#pragma unroll
      for (int ks = 0; ks < 4; ++ks) {
        bf16x8 x = ldsr(bb + (rdX ^ (ks << 5)));
        bf16x8 e0 = ldsr(bb + (rdE ^ (ks << 5)));
        bf16x8 e1 = ldsr(bb + (rdE ^ (ks << 5)) + 4096);
        a0 = MFMA(x, e0, a0);
        a1 = MFMA(x, e1, a1);
      }
    }
    const float sg = nt < 4 ? 1.f : -1.f;
    char* ab = ws + WS_AB + (size_t)nt * (512u << 10) + (size_t)(mt * 8 + wm * 2) * 4096 + (wn * 2) * 1024 + l * 16;
    *(u32x4*)(ab) = swap8(pk8(a0, 0, sg));
    *(u32x4*)(ab + 4096) = swap8(pk8(a0, 8, sg));
    *(u32x4*)(ab + 1024) = swap8(pk8(a1, 0, sg));
    *(u32x4*)(ab + 4096 + 1024) = swap8(pk8(a1, 8, sg));
    STAMPR(10); STAMPR(11); STAMPRT(12); STAMP_FLUSH(0, 13);
  }
}

constexpr uint32_t GBUF = 32768, KV0 = 131072, SLOT = 2080, KVBUF = 4 * SLOT;
#define SMF(a, b, c) __builtin_amdgcn_smfmac_f32_32x32x32_bf16(a, b, c, sidx, 0, 0)
#define LO8(x) __builtin_bit_cast(bf16x16, (f32x8)__builtin_shufflevector(x, x, 0, 1, 2, 3, 4, 5, 6, 7))
#define HI8(x) __builtin_bit_cast(bf16x16, (f32x8)__builtin_shufflevector(x, x, 8, 9, 10, 11, 12, 13, 14, 15))
#define PIN(x) do { asm volatile("" ::"v"(x)); SB; } while (0)
#define EXPR(s, a, b) do { _Pragma("unroll") for (int i_ = a; i_ < b; ++i_) s[i_] = __builtin_amdgcn_exp2f(s[i_]); SB; } while (0)

struct AttnState {
  f32x16 sA, sB, oA, oB, nA, nB;
  bf16x8 qA, qB, kf, v0, v1;
};

template <int BUFI, int U>
DEV void attn_step(AttnState& a, uint32_t aK, uint32_t aV0, uint32_t aV1, int sidx) {
  constexpr uint32_t OFF = BUFI * KVBUF + U * SLOT;
  EXPR(a.sA, 0, 2);
  a.oB = SMF(a.v1, HI8(a.sB), a.oB); PIN(a.oB);
  a.v1 = ldsr(aV1 + OFF); SB;
  EXPR(a.sA, 2, 7);
  a.sB = MFMA(a.kf, a.qB, a.nB); PIN(a.sB);
  a.kf = ldsr(aK + OFF); SB;
  EXPR(a.sA, 7, 12);
  a.oA = SMF(a.v0, LO8(a.sA), a.oA); PIN(a.oA);
  EXPR(a.sA, 12, 16);
  EXPR(a.sB, 0, 2);
  a.oA = SMF(a.v1, HI8(a.sA), a.oA); PIN(a.oA);
  EXPR(a.sB, 2, 7);
  a.sA = MFMA(a.kf, a.qA, a.nA); PIN(a.sA);
  EXPR(a.sB, 7, 12);
  a.oB = SMF(a.v0, LO8(a.sB), a.oB); PIN(a.oB);
  a.v0 = ldsr(aV0 + OFF); SB;
  EXPR(a.sB, 12, 16);
}
template <int BUFI>
DEV void attn_stage(AttnState& a, uint32_t aK, uint32_t aV0, uint32_t aV1, int sidx) {
  barrier_lgkm(); SB;
  attn_step<BUFI, 0>(a, aK, aV0, aV1, sidx);
  attn_step<BUFI, 1>(a, aK, aV0, aV1, sidx);
}

DEV float max16(const f32x16& s) {
  float m = fmaxf(fmaxf(fmaxf(s[0], s[1]), fmaxf(s[2], s[3])), fmaxf(fmaxf(s[4], s[5]), fmaxf(s[6], s[7])));
  float n = fmaxf(fmaxf(fmaxf(s[8], s[9]), fmaxf(s[10], s[11])), fmaxf(fmaxf(s[12], s[13]), fmaxf(s[14], s[15])));
  return fmaxf(m, n);
}
DEV float xhalf_max(float m) {
  auto t = __builtin_amdgcn_permlane32_swap(__float_as_uint(m), __float_as_uint(m), false, false);
  return fmaxf(__uint_as_float(t[0]), __uint_as_float(t[1]));
}
DEV f32x16 splat16(float v) {
  f32x16 r;
#pragma unroll
  for (int i = 0; i < 16; ++i) r[i] = v;
  return r;
}
DEV bf16x8 ones8() { u32x4 o = {0x3F803F80u, 0x3F803F80u, 0x3F803F80u, 0x3F803F80u}; return __builtin_bit_cast(bf16x8, o); }

DEV f32x16 attn_exact(const char* kvh, bf16x8 q, int l, int sidx) {
  const int h = l >> 5, r32 = l & 31;
  float m = -INFINITY;
  const f32x16 z = splat16(0.f);
#pragma unroll 1
  for (int t = 0; t < 64; ++t) {
    bf16x8 k = *(const bf16x8*)(kvh + t * 2048 + l * 16);
    f32x16 s = MFMA(k, q, z);
    m = fmaxf(m, max16(s));
  }
  m = xhalf_max(m);
  const f32x16 nm = splat16(-m);
  f32x16 o = z;
#pragma unroll 1
  for (int t = 0; t < 64; ++t) {
    bf16x8 k = *(const bf16x8*)(kvh + t * 2048 + l * 16);
    bf16x8 v0 = *(const bf16x8*)(kvh + t * 2048 + 1024 + h * 256 + (r32 & 15) * 16);
    bf16x8 v1 = *(const bf16x8*)(kvh + t * 2048 + 1536 + h * 256 + (r32 & 15) * 16);
    if (r32 >= 16) { v0 = ones8(); v1 = ones8(); }
    f32x16 s = MFMA(k, q, nm);
#pragma unroll
    for (int i = 0; i < 16; ++i) s[i] = __builtin_amdgcn_exp2f(s[i]);
    o = SMF(v0, LO8(s), o);
    o = SMF(v1, HI8(s), o);
  }
  return o;
}

__global__ __launch_bounds__(768) void k2_main(const char* __restrict__ cre, const char* __restrict__ cim, char* __restrict__ ws) {
  __shared__ __attribute__((aligned(16))) char smem[KV0 + 3 * KVBUF];
  const uint32_t lds0 = (uint32_t)(size_t)(LDSP char*)smem;
  const int l = threadIdx.x & 63, w = __builtin_amdgcn_readfirstlane(threadIdx.x >> 6);
  const int h = l >> 5, r32 = l & 31;
  const int b = blockIdx.x, xcd = b & 7, idx = b >> 3;
  const int head = xcd * 4 + (idx >> 3), qblk = idx & 7;
  const char* kvh = ws + WS_KV + (size_t)head * 131072;
  STAMP_DECL; STAMPRT(0); STAMPR(1);
  if (w < 8) {
    const int qg = w & 3, kh = w >> 2;
    int sidx = 0xDDDD;
    asm volatile("" : "+v"(sidx));
    if (threadIdx.x < 12) {
      u32x4 o = {0x3F803F80u, 0x3F803F80u, 0x3F803F80u, 0x3F803F80u};
      *(LDSP u32x4*)(size_t)(lds0 + KV0 + threadIdx.x * SLOT + 2048) = o;
    }
    AttnState a;
    const char* qf = ws + WS_Q + ((size_t)head * 64 + qblk * 8 + qg * 2) * 1024 + l * 16;
    a.qA = *(const bf16x8*)qf;
    a.qB = *(const bf16x8*)(qf + 1024);
    const bf16x8 k0 = *(const bf16x8*)(kvh + l * 16);
    a.kf = *(const bf16x8*)(kvh + kh * 65536 + l * 16);
    a.v0 = *(const bf16x8*)(kvh + kh * 65536 + 1024 + h * 256 + (r32 & 15) * 16);
    if (r32 >= 16) a.v0 = ones8();
    {
      const f32x16 z = splat16(0.f);
      f32x16 t0 = MFMA(k0, a.qA, z), t1 = MFMA(k0, a.qB, z);
      a.nA = splat16(-(xhalf_max(max16(t0)) + 64.f));
      a.nB = splat16(-(xhalf_max(max16(t1)) + 64.f));
      a.oA = z; a.oB = z; a.sB = z;
      a.v1 = __builtin_bit_cast(bf16x8, (u32x4){0u, 0u, 0u, 0u});
      a.sA = MFMA(a.kf, a.qA, a.nA);
    }
    const uint32_t sb = lds0 + KV0 + kh * 2 * SLOT;
    const bool onesl = (r32 >= 16 && r32 < 20) || r32 >= 28;
    const uint32_t vrow = h * 256 + (r32 < 16 ? r32 * 16 : 0);
    const uint32_t aK = sb + l * 16;
    const uint32_t aV0 = sb + (onesl ? 2048u : 1024u + vrow);
    const uint32_t aV1 = sb + (onesl ? 2048u : 1536u + vrow);
    STAMPR(2);
#pragma unroll 1
    for (int it = 0; it < 5; ++it) {
      attn_stage<0>(a, aK, aV0, aV1, sidx);
      attn_stage<1>(a, aK, aV0, aV1, sidx);
      attn_stage<2>(a, aK, aV0, aV1, sidx);
    }
    attn_stage<0>(a, aK, aV0, aV1, sidx);
    a.oB = SMF(a.v1, HI8(a.sB), a.oB);
    STAMPR(3);
    const int lr = h ? 12 : 8;
    float lA = h ? a.oA[12] : a.oA[8], lB = h ? a.oB[12] : a.oB[8];
    (void)lr;
    const uint32_t cb = lds0 + qg * 4608 + l * 4;
    if (kh == 1) {
#pragma unroll
      for (int i = 0; i < 8; ++i) {
        *(LDSP float*)(size_t)(cb + i * 256) = a.oA[i];
        *(LDSP float*)(size_t)(cb + (9 + i) * 256) = a.oB[i];
      }
      *(LDSP float*)(size_t)(cb + 8 * 256) = lA;
      *(LDSP float*)(size_t)(cb + 17 * 256) = lB;
    }
    barrier_lgkm();
    if (kh == 0) {
      float oa[8], ob[8];
#pragma unroll
      for (int i = 0; i < 8; ++i) {
        oa[i] = a.oA[i] + *(const LDSP float*)(size_t)(cb + i * 256);
        ob[i] = a.oB[i] + *(const LDSP float*)(size_t)(cb + (9 + i) * 256);
      }
      lA += *(const LDSP float*)(size_t)(cb + 8 * 256);
      lB += *(const LDSP float*)(size_t)(cb + 17 * 256);
      bool okA = lA >= 0x1p-100f && lA <= 0x1p120f, okB = lB >= 0x1p-100f && lB <= 0x1p120f;
#pragma unroll
      for (int i = 0; i < 8; ++i) { okA = okA && (fabsf(oa[i]) <= 0x1p126f); okB = okB && (fabsf(ob[i]) <= 0x1p126f); }
      if (__builtin_amdgcn_ballot_w64(!okA) != 0) {
        f32x16 o = attn_exact(kvh, a.qA, l, sidx);
#pragma unroll
        for (int i = 0; i < 8; ++i) oa[i] = o[i];
        lA = h ? o[12] : o[8];
      }
      if (__builtin_amdgcn_ballot_w64(!okB) != 0) {
        f32x16 o = attn_exact(kvh, a.qB, l, sidx);
#pragma unroll
        for (int i = 0; i < 8; ++i) ob[i] = o[i];
        lB = h ? o[12] : o[8];
      }
      const float rA = 1.0f / lA, rB = 1.0f / lB;
      u32x4 pa, pb;
#pragma unroll
      for (int i = 0; i < 4; ++i) { pa[i] = pk(oa[2 * i] * rA, oa[2 * i + 1] * rA); pb[i] = pk(ob[2 * i] * rB, ob[2 * i + 1] * rB); }
      char* of = ws + WS_O + ((size_t)(qblk * 8 + qg * 2) * 32 + head) * 1024 + l * 16;
      *(u32x4*)of = pa;
      *(u32x4*)(of + 32768) = pb;
    }
    STAMPR(4); STAMPRT(5); STAMP_FLUSH(1, 6);
  } else {
    const int g = w - 8, ge = g & 1, gt = g >> 1;
    const int q = xcd >> 1, mtile = (xcd & 1) * 8 + (idx >> 2), ntile = idx & 3;
    const char* gbase; uint32_t pstride, qstride, vo0, vo1;
    if (g < 2) {
      gbase = (q < 2 ? cre : cim) + (size_t)(mtile * 128 + g * 64) * 4096 + (q & 1) * 2048;
      pstride = 128; qstride = 65536;
      vo0 = (uint32_t)(l >> 3) * 4096u + (uint32_t)(((l & 7) ^ (l >> 4)) * 16);
      vo1 = (uint32_t)(8 + (l >> 3)) * 4096u + (uint32_t)(((l & 7) ^ (4 + (l >> 4))) * 16);
    } else {
      gbase = ws + WS_AB + (size_t)((q >> 1) * 4 + ntile) * (512u << 10) + (size_t)(q & 1) * 64 * 4096 + (g - 2) * 8192;
      pstride = 16384; qstride = 2048;
      vo0 = l * 16; vo1 = l * 16 + 1024;
    }
    const char* kvs = kvh + (size_t)((g >> 1) * 32 + (g & 1)) * 2048;
    const uint32_t voK = l * 16 + 2048, voV = l * 16 + 1024 + (l < 32 ? 2048 : 0);
    auto issueG = [&](int p) {
      const char* sb = gbase + (size_t)p * pstride;
      const uint32_t d = lds0 + (p & 3) * GBUF + g * 8192;
#pragma unroll
      for (int i = 0; i < 8; ++i) glds(d + i * 1024, sb + (size_t)(i >> 1) * qstride, (i & 1) ? vo1 : vo0);
    };
    auto issueKV = [&](int p) {
      const uint32_t d = lds0 + KV0 + (p % 3) * KVBUF + g * SLOT;
      glds(d, kvs + p * 4096, voK);
      glds(d + 1024, kvs + p * 4096, voV);
    };
    issueG(0); issueKV(0); issueG(1); issueKV(1); issueG(2);
    uint32_t rdC[2][4], rdA[2];
#pragma unroll
    for (int ks = 0; ks < 4; ++ks) {
      rdC[0][ks] = lds0 + (((uint32_t)(64 * gt + r32) * 128u + (uint32_t)((h ^ ((r32 >> 1) & 7)) * 16)) ^ (ks << 5));
      rdC[1][ks] = rdC[0][ks] + 65536;
    }
    rdA[0] = lds0 + 16384 + ge * 2048 + l * 16; rdA[1] = rdA[0] + 65536;
    f32x16 c00 = splat16(0.f), c01 = c00, c10 = c00, c11 = c00;
#pragma unroll
    for (int p = 0; p < 16; ++p) {
      if (p == 0) STAMPR(2);
      if (p <= 13) wait_vm<18>(); else if (p == 14) wait_vm<10>(); else wait_vm<0>();
      if (p < 8) STAMPR(22 + p);
      barrier_lgkm();
      STAMPR(3 + p);
      if (p + 2 < 16) issueKV(p + 2);
      if (p + 3 < 16) issueG(p + 3);
      const int hi = (p >> 1) & 1; const uint32_t bo = (p & 1) * GBUF;
#pragma unroll
      for (int ks = 0; ks < 4; ++ks) {
        bf16x8 e0 = ldsr(rdA[hi] + bo + ks * 4096), e1 = ldsr(rdA[hi] + bo + ks * 4096 + 1024);
        bf16x8 t0 = ldsr(rdC[hi][ks] + bo), t1 = ldsr(rdC[hi][ks] + bo + 4096);
        c00 = MFMA(e0, t0, c00); c01 = MFMA(e0, t1, c01);
        c10 = MFMA(e1, t0, c10); c11 = MFMA(e1, t1, c11);
      }
    }
    barrier_lgkm();
    char* pp = ws + WS_P + (size_t)q * (2u << 20) + ((size_t)(mtile * 4 + 2 * gt) * 16 + ntile * 4 + 2 * ge) * 2048 + l * 16;
    *(u32x4*)(pp) = pk8n(c00, 0);                 *(u32x4*)(pp + 1024) = pk8n(c00, 8);
    *(u32x4*)(pp + 32768) = pk8n(c01, 0);         *(u32x4*)(pp + 32768 + 1024) = pk8n(c01, 8);
    *(u32x4*)(pp + 2048) = pk8n(c10, 0);          *(u32x4*)(pp + 2048 + 1024) = pk8n(c10, 8);
    *(u32x4*)(pp + 32768 + 2048) = pk8n(c11, 0);  *(u32x4*)(pp + 32768 + 2048 + 1024) = pk8n(c11, 8);
    STAMPR(19); STAMPR(20); STAMPRT(21); STAMP_FLUSH(1, 30);
  }
}

__global__ __launch_bounds__(256) void k3_out(const char* __restrict__ wo, const char* __restrict__ ws, char* __restrict__ out) {
  __shared__ __attribute__((aligned(16))) char smem[131072];
  const uint32_t lds0 = (uint32_t)(size_t)(LDSP char*)smem;
  const int l = threadIdx.x & 63, w = __builtin_amdgcn_readfirstlane(threadIdx.x >> 6);
  const int h = l >> 5, r32 = l & 31;
  const int b = blockIdx.x, xcd = b & 7, idx = b >> 3;
  const int st = xcd * 4 + (idx >> 3), nt = idx & 7;
  const int w0 = w & 1, w1 = w >> 1;
  STAMP_DECL; STAMPRT(0); STAMPR(1);
  const char* of = ws + WS_O + ((size_t)(st * 2 + w0) * 32 + 4 * w1) * 1024;
  const char* wg = wo + (size_t)(32 * w) * 1024 + nt * 128;
  const uint32_t voO = l * 16;
  const uint32_t voW = (uint32_t)(l >> 3) * 1024u + (uint32_t)(((l & 7) ^ (((l >> 4) & 1) << 2)) * 16);
#pragma unroll
  for (int c = 0; c < 4; ++c) {
#pragma unroll
    for (int i = 0; i < 4; ++i) glds(lds0 + (w0 * 32 + 8 * c + 4 * w1 + i) * 1024, of + (8 * c + i) * 1024, voO);
#pragma unroll
    for (int i = 0; i < 4; ++i) glds(lds0 + 65536 + (128 * c + 32 * w + 8 * i) * 128, wg + (size_t)(128 * c + 8 * i) * 1024, voW);
  }
  const char* pp = ws + WS_P + ((size_t)(st * 2 + w0) * 16 + nt * 2 + w1) * 2048 + l * 16;
  u32x4 pv[4][2];
#pragma unroll
  for (int q = 0; q < 4; ++q) {
    pv[q][0] = *(const u32x4*)(pp + (size_t)q * (2u << 20));
    pv[q][1] = *(const u32x4*)(pp + (size_t)q * (2u << 20) + 1024);
  }
  const int q4 = (l & 15) >> 2, p4 = l & 3, blk = (l >> 4) & 1;
  const uint32_t rdW = lds0 + 65536u + (uint32_t)(4 * h + q4) * 128u + (uint32_t)(((4 * w1 + 2 * blk + (p4 >> 1)) ^ (4 * (q4 >> 1))) * 16 + 8 * (p4 & 1));
  const uint32_t rdO = lds0 + w0 * 32768 + l * 16;
  f32x16 acc = splat16(0.f);
#pragma unroll
  for (int c = 0; c < 4; ++c) {
    if (c == 0) STAMPR(2);
    if (c == 0) wait_vm<32>(); else if (c == 1) wait_vm<24>(); else if (c == 2) wait_vm<16>(); else wait_vm<8>();
    barrier_lgkm();
    STAMPR(3 + c);
#pragma unroll
    for (int i = 0; i < 8; ++i) {
      const int hd = 8 * c + i;
      bf16x8 o = ldsr(rdO + hd * 1024);
      bf16x8 wf = ldstr(rdW + hd * 2048, rdW + hd * 2048 + 1024);
      acc = MFMA(wf, o, acc);
    }
  }
  char* orow = out + (size_t)(st * 64 + w0 * 32 + r32) * 2048 + (nt * 64 + w1 * 32 + 8 * h) * 2;
  *(u32x4*)(orow) = swap8(pk8n(acc, 0));
  *(u32x4*)(orow + 32) = swap8(pk8n(acc, 8));
#pragma unroll
  for (int r = 0; r < 2; ++r) {
    u32x4 o;
#pragma unroll
    for (int i = 0; i < 4; ++i) {
      float lo = 0.f, hi = 0.f;
#pragma unroll
      for (int q = 0; q < 4; ++q) { lo += __uint_as_float(pv[q][r][i] << 16); hi += __uint_as_float(pv[q][r][i] & 0xffff0000u); }
      o[i] = pk(lo, hi);
    }
    *(u32x4*)(orow + 1024 + 32 * r) = swap8(o);
  }
  STAMPR(7); STAMPRT(8); STAMP_FLUSH(2, 9);
}

__global__ __launch_bounds__(256) void k4_diag(char* ws) {
  const unsigned long long* d = (const unsigned long long*)(ws + WS_DBG);
  __shared__ unsigned long long sm[16][256];
  const int b = threadIdx.x;
  auto Sx = [&](int k, int w, int i) { return d[(((size_t)k * 256 + b) * 16 + w) * 32 + i]; };
  sm[0][b] = Sx(0, 0, 0); sm[1][b] = Sx(0, 0, 12);
  sm[2][b] = Sx(1, 0, 0); sm[3][b] = Sx(1, 0, 5);
  sm[4][b] = Sx(2, 0, 0); sm[5][b] = Sx(2, 0, 8);
  sm[6][b] = Sx(0, 0, 2) - Sx(0, 0, 1);
  sm[7][b] = Sx(0, 0, 11) - Sx(0, 0, 1);
  sm[8][b] = Sx(1, 8, 3) - Sx(1, 8, 1);
  sm[9][b] = Sx(1, 0, 3) - Sx(1, 0, 2);
  sm[10][b] = Sx(1, 0, 4) - Sx(1, 0, 1);
  sm[11][b] = Sx(2, 0, 3) - Sx(2, 0, 1);
  sm[12][b] = Sx(2, 0, 7) - Sx(2, 0, 1);
  __syncthreads();
  if (b == 0) {
    unsigned long long mn[6], mx[6]; double av[7];
    for (int i = 0; i < 6; ++i) { mn[i] = ~0ull; mx[i] = 0; for (int j = 0; j < 256; ++j) { mn[i] = sm[i][j] < mn[i] ? sm[i][j] : mn[i]; mx[i] = sm[i][j] > mx[i] ? sm[i][j] : mx[i]; } }
    for (int i = 0; i < 7; ++i) { double s = 0; for (int j = 0; j < 256; ++j) s += (double)sm[6 + i][j]; av[i] = s / 256; }
    unsigned probe = *(const unsigned*)(ws + (100u << 20));
    unsigned mark = *(const unsigned*)(ws + (101u << 20));
    *(unsigned*)(ws + (101u << 20)) = 0x13572468u;
    const unsigned long long t0 = mn[0];
    printf("DG %s %08x K1 %d..%d e %d..%d K2 %d..%d e %d..%d K3 %d..%d e %d..%d | %d %d | %d %d %d | %d %d\n", mark == 0x13572468u ? "W" : "R", probe,
           (int)(mn[0] - t0), (int)(mx[0] - t0), (int)(mn[1] - t0), (int)(mx[1] - t0), (int)(mn[2] - t0), (int)(mx[2] - t0), (int)(mn[3] - t0), (int)(mx[3] - t0),
           (int)(mn[4] - t0), (int)(mx[4] - t0), (int)(mn[5] - t0), (int)(mx[5] - t0),
           (int)av[0], (int)av[1], (int)av[2], (int)av[3], (int)av[4], (int)av[5], (int)av[6]);
  }
}

extern "C" void kernel_launch(void* const* d_in, const int* in_sizes, int n_in, void* d_out, int out_size, void* d_ws, size_t ws_size,
                              hipStream_t stream) {
  (void)in_sizes; (void)n_in; (void)out_size; (void)ws_size;
  k1_proj<<<256, 512, 0, stream>>>((const char*)d_in[0], (const char*)d_in[1], (const char*)d_in[3], (const char*)d_in[6],
                                   (const char*)d_in[7], (char*)d_ws);
  k2_main<<<256, 768, 0, stream>>>((const char*)d_in[4], (const char*)d_in[5], (char*)d_ws);
  k3_out<<<256, 256, 0, stream>>>((const char*)d_in[2], (const char*)d_ws, (char*)d_out);
  k4_diag<<<1, 256, 0, stream>>>((char*)d_ws);
}
```

```cpp
#include <hip/hip_runtime.h>
#include <stdint.h>
#define STAMP 1
#define QSEL 0
#define K1E 12
#define K1C 11

typedef __bf16 bf16x8  __attribute__((ext_vector_type(8)));
typedef __bf16 bf16x16 __attribute__((ext_vector_type(16)));
typedef __bf16 bf16x2  __attribute__((ext_vector_type(2)));
typedef short  s16x4   __attribute__((ext_vector_type(4)));
typedef short  s16x8   __attribute__((ext_vector_type(8)));
typedef float  f32x2   __attribute__((ext_vector_type(2)));
typedef float  f32x8   __attribute__((ext_vector_type(8)));
typedef float  f32x16  __attribute__((ext_vector_type(16)));
typedef unsigned u32x4 __attribute__((ext_vector_type(4)));
typedef unsigned u32x2 __attribute__((ext_vector_type(2)));
#define LDSP __attribute__((address_space(3)))
#define DEV __device__ __forceinline__

constexpr int S = 2048, D = 512;
constexpr size_t WS_Q  = 0;
constexpr size_t WS_KV = 2u << 20;
constexpr size_t WS_AB = (6u << 20) + 65536;
constexpr size_t WS_O  = WS_AB + (4u << 20);
constexpr size_t WS_P  = WS_O + (2u << 20);

constexpr size_t WS_DBG = 64u << 20;
#define STAMP_DECL unsigned long long st_[32]
#define STAMPR(i) do { SB; st_[i] = __builtin_amdgcn_s_memtime(); SB; } while (0)
#define STAMPRT(i) do { st_[i] = __builtin_amdgcn_s_memrealtime(); } while (0)
#define STAMP_FLUSH(kern, n) do { if ((threadIdx.x & 63) == 0) { unsigned long long* d_ = (unsigned long long*)((char*)ws + WS_DBG) + ((((size_t)(kern) * 256 + blockIdx.x) * 16 + (threadIdx.x >> 6)) * 32); _Pragma("unroll") for (int i_ = 0; i_ < n; ++i_) d_[i_] = st_[i_]; } } while (0)
#define SB __builtin_amdgcn_sched_barrier(0)
#define MFMA(a, b, c) __builtin_amdgcn_mfma_f32_32x32x16_bf16(a, b, c, 0, 0, 0)

DEV void glds(uint32_t ldsaddr, const void* sbase, uint32_t voff) {
  asm volatile("s_mov_b32 m0, %2\n\ts_nop 0\n\tglobal_load_lds_dwordx4 %0, %1" ::"v"(voff), "s"(sbase), "s"(ldsaddr) : "memory");
}
template <int N> DEV void wait_vm() { asm volatile("s_waitcnt vmcnt(%0)" ::"n"(N) : "memory"); }
DEV void barrier_lgkm() { asm volatile("s_waitcnt lgkmcnt(0)\n\ts_barrier" ::: "memory"); }
DEV bf16x8 ldsr(uint32_t a) { return *(const LDSP bf16x8*)(size_t)a; }
DEV bf16x8 ldstr(uint32_t a0, uint32_t a1) {
  s16x4 x = __builtin_amdgcn_ds_read_tr16_b64_v4i16((LDSP s16x4*)(size_t)a0);
  s16x4 y = __builtin_amdgcn_ds_read_tr16_b64_v4i16((LDSP s16x4*)(size_t)a1);
  return __builtin_bit_cast(bf16x8, (s16x8)__builtin_shufflevector(x, y, 0, 1, 2, 3, 4, 5, 6, 7));
}
DEV unsigned pk(float a, float b) {
  f32x2 v = {a, b};
  return __builtin_bit_cast(unsigned, __builtin_convertvector(v, bf16x2));
}
DEV u32x4 pk8(const f32x16& a, int o, float sc) {
  u32x4 r;
  r[0] = pk(a[o + 0] * sc, a[o + 1] * sc); r[1] = pk(a[o + 2] * sc, a[o + 3] * sc);
  r[2] = pk(a[o + 4] * sc, a[o + 5] * sc); r[3] = pk(a[o + 6] * sc, a[o + 7] * sc);
  return r;
}
DEV u32x4 pk8n(const f32x16& a, int o) {
  u32x4 r;
  r[0] = pk(a[o + 0], a[o + 1]); r[1] = pk(a[o + 2], a[o + 3]);
  r[2] = pk(a[o + 4], a[o + 5]); r[3] = pk(a[o + 6], a[o + 7]);
  return r;
}
DEV u32x4 swap8(u32x4 v) {
  auto a = __builtin_amdgcn_permlane32_swap(v[0], v[2], false, false);
  auto b = __builtin_amdgcn_permlane32_swap(v[1], v[3], false, false);
  u32x4 r; r[0] = a[0]; r[1] = b[0]; r[2] = a[1]; r[3] = b[1];
  return r;
}

__global__ __launch_bounds__(512) void k1_proj(const char* __restrict__ xA, const char* __restrict__ wqkv, const char* __restrict__ xB,
                                               const char* __restrict__ hre, const char* __restrict__ him, char* __restrict__ ws) {
  __shared__ __attribute__((aligned(16))) char smem[163840];
  const uint32_t lds0 = (uint32_t)(size_t)(LDSP char*)smem;
  const int l = threadIdx.x & 63, w = __builtin_amdgcn_readfirstlane(threadIdx.x >> 6);
  const int h = l >> 5, r32 = l & 31;
  const int b = blockIdx.x, xcd = b & 7, idx = b >> 3;
  const int role = idx >> 4, i16 = idx & 15;
  const int mt = (xcd >> 1) * 4 + (i16 >> 2), nt = (xcd & 1) * 4 + (i16 & 3);
  const int wm = w & 3, wn = w >> 2;
  STAMP_DECL; STAMPRT(0); STAMPR(1);
  const uint32_t xsw = (uint32_t)(((l & 7) ^ ((4 * (w & 1) + (l >> 4)) & 7)) * 16);
  const uint32_t voffX = (uint32_t)(8 * w + (l >> 3)) * 1024u + xsw;
  const uint32_t rdX = (uint32_t)(32 * wm + r32) * 128u + (uint32_t)((h ^ ((r32 >> 1) & 7)) * 16);
  if (role == 0) {
    constexpr uint32_t BUF = 40960;
    const char* gx = xA + (size_t)mt * 128 * 1024;
    const char* gw = wqkv + nt * 128;
    const uint32_t voffW = (uint32_t)(8 * w + (l >> 3)) * 3072u + (uint32_t)(((l & 7) ^ (((l >> 4) & 1) << 2)) * 16);
    auto issue = [&](int p) {
      const uint32_t bb = lds0 + (p & 3) * BUF;
      glds(bb + w * 1024, gx + p * 128, voffX);
      glds(bb + (w + 8) * 1024, gx + p * 128 + 65536, voffX);
#pragma unroll
      for (int g = 0; g < 3; ++g) glds(bb + 16384 + g * 8192 + w * 1024, gw + (size_t)p * 64 * 3072 + g * 1024, voffW);
    };
    issue(0); issue(1); issue(2); issue(3);
    const int q = (l & 15) >> 2, pp = l & 3, blk = (l >> 4) & 1;
    const uint32_t rdW = 16384u + (uint32_t)(8 * h + q) * 128u + (uint32_t)(((4 * wn + 2 * blk + (pp >> 1)) ^ (4 * (q >> 1))) * 16 + 8 * (pp & 1));
    f32x16 aq, ak, av;
#pragma unroll
    for (int i = 0; i < 16; ++i) { aq[i] = 0.f; ak[i] = 0.f; av[i] = 0.f; }
#pragma unroll
    for (int p = 0; p < 8; ++p) {
      if (p == 0) wait_vm<15>(); else if (p <= 5) wait_vm<10>(); else if (p == 6) wait_vm<5>(); else wait_vm<0>();
      barrier_lgkm();
      STAMPR(2 + p);
      if (p >= 1 && p + 3 < 8) issue(p + 3);
      const uint32_t bb = lds0 + (p & 3) * BUF;
#pragma unroll
      for (int ks = 0; ks < 4; ++ks) {
        bf16x8 x = ldsr(bb + (rdX ^ (ks << 5)));
        bf16x8 wq = ldstr(bb + rdW + ks * 2048, bb + rdW + ks * 2048 + 512);
        bf16x8 wk = ldstr(bb + rdW + 8192 + ks * 2048, bb + rdW + 8192 + ks * 2048 + 512);
        bf16x8 wv = ldstr(bb + rdW + 16384 + ks * 2048, bb + rdW + 16384 + ks * 2048 + 512);
        aq = MFMA(wq, x, aq);
        ak = MFMA(wk, x, ak);
        av = MFMA(x, wv, av);
      }
    }
    const int tile = mt * 4 + wm;
    const int head0 = nt * 4 + wn * 2;
    constexpr float QS = 0.25f * 1.4426950408889634f;
    char* qf = ws + WS_Q;
    char* kv = ws + WS_KV;
    const int pr = (r32 & 0x13) | ((r32 & 4) << 1) | ((r32 & 8) >> 1);
#pragma unroll
    for (int hh = 0; hh < 2; ++hh) {
      *(u32x4*)(qf + ((size_t)(head0 + hh) * 64 + tile) * 1024 + l * 16) = pk8(aq, 8 * hh, QS);
      *(u32x4*)(kv + ((size_t)(head0 + hh) * 64 + tile) * 2048 + (h * 32 + pr) * 16) = pk8n(ak, 8 * hh);
    }
    char* vb = kv + ((size_t)(head0 + (r32 >> 4)) * 64 + tile) * 2048 + 1024 + h * 256 + (r32 & 15) * 16;
    STAMPR(10);
    *(u32x4*)(vb) = pk8n(av, 0);
    *(u32x4*)(vb + 512) = pk8n(av, 8);
    STAMPR(11); STAMPRT(12); STAMP_FLUSH(0, 13);
  } else {
    constexpr uint32_t BUF = 32768;
    const char* gx = xB + (size_t)mt * 128 * 1024;
    const char* gw = (nt < 4 ? hre : him) + (size_t)(nt & 3) * 128 * 1024;
    auto issue = [&](int p) {
      const uint32_t bb = lds0 + (p & 3) * BUF;
      glds(bb + w * 1024, gx + p * 128, voffX);
      glds(bb + (w + 8) * 1024, gx + p * 128 + 65536, voffX);
      glds(bb + 16384 + w * 1024, gw + p * 128, voffX);
      glds(bb + 16384 + (w + 8) * 1024, gw + p * 128 + 65536, voffX);
    };
    issue(0); issue(1); issue(2); issue(3);
    const uint32_t rdE = 16384u + (uint32_t)(64 * wn + r32) * 128u + (uint32_t)((h ^ ((r32 >> 1) & 7)) * 16);
    f32x16 a0, a1;
#pragma unroll
    for (int i = 0; i < 16; ++i) { a0[i] = 0.f; a1[i] = 0.f; }
#pragma unroll
    for (int p = 0; p < 8; ++p) {
      if (p == 0) wait_vm<12>(); else if (p <= 5) wait_vm<8>(); else if (p == 6) wait_vm<4>(); else wait_vm<0>();
      barrier_lgkm();
      STAMPR(2 + p);
      if (p >= 1 && p + 3 < 8) issue(p + 3);
      const uint32_t bb = lds0 + (p & 3) * BUF;
#pragma unroll
      for (int ks = 0; ks < 4; ++ks) {
        bf16x8 x = ldsr(bb + (rdX ^ (ks << 5)));
        bf16x8 e0 = ldsr(bb + (rdE ^ (ks << 5)));
        bf16x8 e1 = ldsr(bb + (rdE ^ (ks << 5)) + 4096);
        a0 = MFMA(x, e0, a0);
        a1 = MFMA(x, e1, a1);
      }
    }
    const float sg = nt < 4 ? 1.f : -1.f;
    char* ab = ws + WS_AB + (size_t)nt * (512u << 10) + (size_t)(mt * 8 + wm * 2) * 4096 + (wn * 2) * 1024 + l * 16;
    *(u32x4*)(ab) = swap8(pk8(a0, 0, sg));
    *(u32x4*)(ab + 4096) = swap8(pk8(a0, 8, sg));
    *(u32x4*)(ab + 1024) = swap8(pk8(a1, 0, sg));
    *(u32x4*)(ab + 4096 + 1024) = swap8(pk8(a1, 8, sg));
    STAMPR(10); STAMPR(11); STAMPRT(12); STAMP_FLUSH(0, 13);
  }
}

constexpr uint32_t GBUF = 32768, KV0 = 131072, SLOT = 2080, KVBUF = 4 * SLOT;
#define SMF(a, b, c) __builtin_amdgcn_smfmac_f32_32x32x32_bf16(a, b, c, sidx, 0, 0)
#define LO8(x) __builtin_bit_cast(bf16x16, (f32x8)__builtin_shufflevector(x, x, 0, 1, 2, 3, 4, 5, 6, 7))
#define HI8(x) __builtin_bit_cast(bf16x16, (f32x8)__builtin_shufflevector(x, x, 8, 9, 10, 11, 12, 13, 14, 15))
#define PIN(x) do { asm volatile("" ::"v"(x)); SB; } while (0)
#define EXPR(s, a, b) do { _Pragma("unroll") for (int i_ = a; i_ < b; ++i_) s[i_] = __builtin_amdgcn_exp2f(s[i_]); SB; } while (0)

struct AttnState {
  f32x16 sA, sB, oA, oB, nA, nB;
  bf16x8 qA, qB, kf, v0, v1;
};

template <int BUFI, int U>
DEV void attn_step(AttnState& a, uint32_t aK, uint32_t aV0, uint32_t aV1, int sidx) {
  constexpr uint32_t OFF = BUFI * KVBUF + U * SLOT;
  EXPR(a.sA, 0, 2);
  a.oB = SMF(a.v1, HI8(a.sB), a.oB); PIN(a.oB);
  a.v1 = ldsr(aV1 + OFF); SB;
  EXPR(a.sA, 2, 7);
  a.sB = MFMA(a.kf, a.qB, a.nB); PIN(a.sB);
  a.kf = ldsr(aK + OFF); SB;
  EXPR(a.sA, 7, 12);
  a.oA = SMF(a.v0, LO8(a.sA), a.oA); PIN(a.oA);
  EXPR(a.sA, 12, 16);
  EXPR(a.sB, 0, 2);
  a.oA = SMF(a.v1, HI8(a.sA), a.oA); PIN(a.oA);
  EXPR(a.sB, 2, 7);
  a.sA = MFMA(a.kf, a.qA, a.nA); PIN(a.sA);
  EXPR(a.sB, 7, 12);
  a.oB = SMF(a.v0, LO8(a.sB), a.oB); PIN(a.oB);
  a.v0 = ldsr(aV0 + OFF); SB;
  EXPR(a.sB, 12, 16);
}
template <int BUFI>
DEV void attn_stage(AttnState& a, uint32_t aK, uint32_t aV0, uint32_t aV1, int sidx) {
  barrier_lgkm(); SB;
  attn_step<BUFI, 0>(a, aK, aV0, aV1, sidx);
  attn_step<BUFI, 1>(a, aK, aV0, aV1, sidx);
}

DEV float max16(const f32x16& s) {
  float m = fmaxf(fmaxf(fmaxf(s[0], s[1]), fmaxf(s[2], s[3])), fmaxf(fmaxf(s[4], s[5]), fmaxf(s[6], s[7])));
  float n = fmaxf(fmaxf(fmaxf(s[8], s[9]), fmaxf(s[10], s[11])), fmaxf(fmaxf(s[12], s[13]), fmaxf(s[14], s[15])));
  return fmaxf(m, n);
}
DEV float xhalf_max(float m) {
  auto t = __builtin_amdgcn_permlane32_swap(__float_as_uint(m), __float_as_uint(m), false, false);
  return fmaxf(__uint_as_float(t[0]), __uint_as_float(t[1]));
}
DEV f32x16 splat16(float v) {
  f32x16 r;
#pragma unroll
  for (int i = 0; i < 16; ++i) r[i] = v;
  return r;
}
DEV bf16x8 ones8() { u32x4 o = {0x3F803F80u, 0x3F803F80u, 0x3F803F80u, 0x3F803F80u}; return __builtin_bit_cast(bf16x8, o); }

DEV f32x16 attn_exact(const char* kvh, bf16x8 q, int l, int sidx) {
  const int h = l >> 5, r32 = l & 31;
  float m = -INFINITY;
  const f32x16 z = splat16(0.f);
#pragma unroll 1
  for (int t = 0; t < 64; ++t) {
    bf16x8 k = *(const bf16x8*)(kvh + t * 2048 + l * 16);
    f32x16 s = MFMA(k, q, z);
    m = fmaxf(m, max16(s));
  }
  m = xhalf_max(m);
  const f32x16 nm = splat16(-m);
  f32x16 o = z;
#pragma unroll 1
  for (int t = 0; t < 64; ++t) {
    bf16x8 k = *(const bf16x8*)(kvh + t * 2048 + l * 16);
    bf16x8 v0 = *(const bf16x8*)(kvh + t * 2048 + 1024 + h * 256 + (r32 & 15) * 16);
    bf16x8 v1 = *(const bf16x8*)(kvh + t * 2048 + 1536 + h * 256 + (r32 & 15) * 16);
    if (r32 >= 16) { v0 = ones8(); v1 = ones8(); }
    f32x16 s = MFMA(k, q, nm);
#pragma unroll
    for (int i = 0; i < 16; ++i) s[i] = __builtin_amdgcn_exp2f(s[i]);
    o = SMF(v0, LO8(s), o);
    o = SMF(v1, HI8(s), o);
  }
  return o;
}

__global__ __launch_bounds__(768) void k2_main(const char* __restrict__ cre, const char* __restrict__ cim, char* __restrict__ ws) {
  __shared__ __attribute__((aligned(16))) char smem[KV0 + 3 * KVBUF];
  const uint32_t lds0 = (uint32_t)(size_t)(LDSP char*)smem;
  const int l = threadIdx.x & 63, w = __builtin_amdgcn_readfirstlane(threadIdx.x >> 6);
  const int h = l >> 5, r32 = l & 31;
  const int b = blockIdx.x, xcd = b & 7, idx = b >> 3;
  const int head = xcd * 4 + (idx >> 3), qblk = idx & 7;
  const char* kvh = ws + WS_KV + (size_t)head * 131072;
  STAMP_DECL; STAMPRT(0); STAMPR(1);
  if (w < 8) {
    const int qg = w & 3, kh = w >> 2;
    int sidx = 0xDDDD;
    asm volatile("" : "+v"(sidx));
    if (threadIdx.x < 12) {
      u32x4 o = {0x3F803F80u, 0x3F803F80u, 0x3F803F80u, 0x3F803F80u};
      *(LDSP u32x4*)(size_t)(lds0 + KV0 + threadIdx.x * SLOT + 2048) = o;
    }
    AttnState a;
    const char* qf = ws + WS_Q + ((size_t)head * 64 + qblk * 8 + qg * 2) * 1024 + l * 16;
    a.qA = *(const bf16x8*)qf;
    a.qB = *(const bf16x8*)(qf + 1024);
    const bf16x8 k0 = *(const bf16x8*)(kvh + l * 16);
    a.kf = *(const bf16x8*)(kvh + kh * 65536 + l * 16);
    a.v0 = *(const bf16x8*)(kvh + kh * 65536 + 1024 + h * 256 + (r32 & 15) * 16);
    if (r32 >= 16) a.v0 = ones8();
    {
      const f32x16 z = splat16(0.f);
      f32x16 t0 = MFMA(k0, a.qA, z), t1 = MFMA(k0, a.qB, z);
      a.nA = splat16(-(xhalf_max(max16(t0)) + 64.f));
      a.nB = splat16(-(xhalf_max(max16(t1)) + 64.f));
      a.oA = z; a.oB = z; a.sB = z;
      a.v1 = __builtin_bit_cast(bf16x8, (u32x4){0u, 0u, 0u, 0u});
      a.sA = MFMA(a.kf, a.qA, a.nA);
    }
    const uint32_t sb = lds0 + KV0 + kh * 2 * SLOT;
    const bool onesl = (r32 >= 16 && r32 < 20) || r32 >= 28;
    const uint32_t vrow = h * 256 + (r32 < 16 ? r32 * 16 : 0);
    const uint32_t aK = sb + l * 16;
    const uint32_t aV0 = sb + (onesl ? 2048u : 1024u + vrow);
    const uint32_t aV1 = sb + (onesl ? 2048u : 1536u + vrow);
    STAMPR(2);
#pragma unroll 1
    for (int it = 0; it < 5; ++it) {
      attn_stage<0>(a, aK, aV0, aV1, sidx);
      attn_stage<1>(a, aK, aV0, aV1, sidx);
      attn_stage<2>(a, aK, aV0, aV1, sidx);
    }
    attn_stage<0>(a, aK, aV0, aV1, sidx);
    a.oB = SMF(a.v1, HI8(a.sB), a.oB);
    STAMPR(3);
    const int lr = h ? 12 : 8;
    float lA = h ? a.oA[12] : a.oA[8], lB = h ? a.oB[12] : a.oB[8];
    (void)lr;
    const uint32_t cb = lds0 + qg * 4608 + l * 4;
    if (kh == 1) {
#pragma unroll
      for (int i = 0; i < 8; ++i) {
        *(LDSP float*)(size_t)(cb + i * 256) = a.oA[i];
        *(LDSP float*)(size_t)(cb + (9 + i) * 256) = a.oB[i];
      }
      *(LDSP float*)(size_t)(cb + 8 * 256) = lA;
      *(LDSP float*)(size_t)(cb + 17 * 256) = lB;
    }
    barrier_lgkm();
    if (kh == 0) {
      float oa[8], ob[8];
#pragma unroll
      for (int i = 0; i < 8; ++i) {
        oa[i] = a.oA[i] + *(const LDSP float*)(size_t)(cb + i * 256);
        ob[i] = a.oB[i] + *(const LDSP float*)(size_t)(cb + (9 + i) * 256);
      }
      lA += *(const LDSP float*)(size_t)(cb + 8 * 256);
      lB += *(const LDSP float*)(size_t)(cb + 17 * 256);
      bool okA = lA >= 0x1p-100f && lA <= 0x1p120f, okB = lB >= 0x1p-100f && lB <= 0x1p120f;
#pragma unroll
      for (int i = 0; i < 8; ++i) { okA = okA && (fabsf(oa[i]) <= 0x1p126f); okB = okB && (fabsf(ob[i]) <= 0x1p126f); }
      if (__builtin_amdgcn_ballot_w64(!okA) != 0) {
        f32x16 o = attn_exact(kvh, a.qA, l, sidx);
#pragma unroll
        for (int i = 0; i < 8; ++i) oa[i] = o[i];
        lA = h ? o[12] : o[8];
      }
      if (__builtin_amdgcn_ballot_w64(!okB) != 0) {
        f32x16 o = attn_exact(kvh, a.qB, l, sidx);
#pragma unroll
        for (int i = 0; i < 8; ++i) ob[i] = o[i];
        lB = h ? o[12] : o[8];
      }
      const float rA = 1.0f / lA, rB = 1.0f / lB;
      u32x4 pa, pb;
#pragma unroll
      for (int i = 0; i < 4; ++i) { pa[i] = pk(oa[2 * i] * rA, oa[2 * i + 1] * rA); pb[i] = pk(ob[2 * i] * rB, ob[2 * i + 1] * rB); }
      char* of = ws + WS_O + ((size_t)(qblk * 8 + qg * 2) * 32 + head) * 1024 + l * 16;
      *(u32x4*)of = pa;
      *(u32x4*)(of + 32768) = pb;
    }
    STAMPR(4); STAMPRT(5); STAMP_FLUSH(1, 6);
  } else {
    const int g = w - 8, ge = g & 1, gt = g >> 1;
    const int q = xcd >> 1, mtile = (xcd & 1) * 8 + (idx >> 2), ntile = idx & 3;
    const char* gbase; uint32_t pstride, qstride, vo0, vo1;
    if (g < 2) {
      gbase = (q < 2 ? cre : cim) + (size_t)(mtile * 128 + g * 64) * 4096 + (q & 1) * 2048;
      pstride = 128; qstride = 65536;
      vo0 = (uint32_t)(l >> 3) * 4096u + (uint32_t)(((l & 7) ^ (l >> 4)) * 16);
      vo1 = (uint32_t)(8 + (l >> 3)) * 4096u + (uint32_t)(((l & 7) ^ (4 + (l >> 4))) * 16);
    } else {
      gbase = ws + WS_AB + (size_t)((q >> 1) * 4 + ntile) * (512u << 10) + (size_t)(q & 1) * 64 * 4096 + (g - 2) * 8192;
      pstride = 16384; qstride = 2048;
      vo0 = l * 16; vo1 = l * 16 + 1024;
    }
    const char* kvs = kvh + (size_t)((g >> 1) * 32 + (g & 1)) * 2048;
    const uint32_t voK = l * 16 + 2048, voV = l * 16 + 1024 + (l < 32 ? 2048 : 0);
    auto issueG = [&](int p) {
      const char* sb = gbase + (size_t)p * pstride;
      const uint32_t d = lds0 + (p & 3) * GBUF + g * 8192;
#pragma unroll
      for (int i = 0; i < 8; ++i) glds(d + i * 1024, sb + (size_t)(i >> 1) * qstride, (i & 1) ? vo1 : vo0);
    };
    auto issueKV = [&](int p) {
      const uint32_t d = lds0 + KV0 + (p % 3) * KVBUF + g * SLOT;
      glds(d, kvs + p * 4096, voK);
      glds(d + 1024, kvs + p * 4096, voV);
    };
    issueG(0); issueKV(0); issueG(1); issueKV(1); issueG(2);
    uint32_t rdC[2][4], rdA[2];
#pragma unroll
    for (int ks = 0; ks < 4; ++ks) {
      rdC[0][ks] = lds0 + (((uint32_t)(64 * gt + r32) * 128u + (uint32_t)((h ^ ((r32 >> 1) & 7)) * 16)) ^ (ks << 5));
      rdC[1][ks] = rdC[0][ks] + 65536;
    }
    rdA[0] = lds0 + 16384 + ge * 2048 + l * 16; rdA[1] = rdA[0] + 65536;
    f32x16 c00 = splat16(0.f), c01 = c00, c10 = c00, c11 = c00;
#pragma unroll
    for (int p = 0; p < 16; ++p) {
      if (p == 0) STAMPR(2);
      if (p <= 13) wait_vm<18>(); else if (p == 14) wait_vm<10>(); else wait_vm<0>();
      if (p < 8) STAMPR(22 + p);
      barrier_lgkm();
      STAMPR(3 + p);
      if (p + 2 < 16) issueKV(p + 2);
      if (p + 3 < 16) issueG(p + 3);
      const int hi = (p >> 1) & 1; const uint32_t bo = (p & 1) * GBUF;
#pragma unroll
      for (int ks = 0; ks < 4; ++ks) {
        bf16x8 e0 = ldsr(rdA[hi] + bo + ks * 4096), e1 = ldsr(rdA[hi] + bo + ks * 4096 + 1024);
        bf16x8 t0 = ldsr(rdC[hi][ks] + bo), t1 = ldsr(rdC[hi][ks] + bo + 4096);
        c00 = MFMA(e0, t0, c00); c01 = MFMA(e0, t1, c01);
        c10 = MFMA(e1, t0, c10); c11 = MFMA(e1, t1, c11);
      }
    }
    barrier_lgkm();
    char* pp = ws + WS_P + (size_t)q * (2u << 20) + ((size_t)(mtile * 4 + 2 * gt) * 16 + ntile * 4 + 2 * ge) * 2048 + l * 16;
    *(u32x4*)(pp) = pk8n(c00, 0);                 *(u32x4*)(pp + 1024) = pk8n(c00, 8);
    *(u32x4*)(pp + 32768) = pk8n(c01, 0);         *(u32x4*)(pp + 32768 + 1024) = pk8n(c01, 8);
    *(u32x4*)(pp + 2048) = pk8n(c10, 0);          *(u32x4*)(pp + 2048 + 1024) = pk8n(c10, 8);
    *(u32x4*)(pp + 32768 + 2048) = pk8n(c11, 0);  *(u32x4*)(pp + 32768 + 2048 + 1024) = pk8n(c11, 8);
    STAMPR(19); STAMPR(20); STAMPRT(21); STAMP_FLUSH(1, 30);
  }
}

__global__ __launch_bounds__(256) void k3_out(const char* __restrict__ wo, const char* __restrict__ ws, char* __restrict__ out) {
  __shared__ __attribute__((aligned(16))) char smem[131072];
  const uint32_t lds0 = (uint32_t)(size_t)(LDSP char*)smem;
  const int l = threadIdx.x & 63, w = __builtin_amdgcn_readfirstlane(threadIdx.x >> 6);
  const int h = l >> 5, r32 = l & 31;
  const int b = blockIdx.x, xcd = b & 7, idx = b >> 3;
  const int st = xcd * 4 + (idx >> 3), nt = idx & 7;
  const int w0 = w & 1, w1 = w >> 1;
  STAMP_DECL; STAMPRT(0); STAMPR(1);
  const char* of = ws + WS_O + ((size_t)(st * 2 + w0) * 32 + 4 * w1) * 1024;
  const char* wg = wo + (size_t)(32 * w) * 1024 + nt * 128;
  const uint32_t voO = l * 16;
  const uint32_t voW = (uint32_t)(l >> 3) * 1024u + (uint32_t)(((l & 7) ^ (((l >> 4) & 1) << 2)) * 16);
#pragma unroll
  for (int c = 0; c < 4; ++c) {
#pragma unroll
    for (int i = 0; i < 4; ++i) glds(lds0 + (w0 * 32 + 8 * c + 4 * w1 + i) * 1024, of + (8 * c + i) * 1024, voO);
#pragma unroll
    for (int i = 0; i < 4; ++i) glds(lds0 + 65536 + (128 * c + 32 * w + 8 * i) * 128, wg + (size_t)(128 * c + 8 * i) * 1024, voW);
  }
  const char* pp = ws + WS_P + ((size_t)(st * 2 + w0) * 16 + nt * 2 + w1) * 2048 + l * 16;
  u32x4 pv[4][2];
#pragma unroll
  for (int q = 0; q < 4; ++q) {
    pv[q][0] = *(const u32x4*)(pp + (size_t)q * (2u << 20));
    pv[q][1] = *(const u32x4*)(pp + (size_t)q * (2u << 20) + 1024);
  }
  const int q4 = (l & 15) >> 2, p4 = l & 3, blk = (l >> 4) & 1;
  const uint32_t rdW = lds0 + 65536u + (uint32_t)(4 * h + q4) * 128u + (uint32_t)(((4 * w1 + 2 * blk + (p4 >> 1)) ^ (4 * (q4 >> 1))) * 16 + 8 * (p4 & 1));
  const uint32_t rdO = lds0 + w0 * 32768 + l * 16;
  f32x16 acc = splat16(0.f);
#pragma unroll
  for (int c = 0; c < 4; ++c) {
    if (c == 0) STAMPR(2);
    if (c == 0) wait_vm<32>(); else if (c == 1) wait_vm<24>(); else if (c == 2) wait_vm<16>(); else wait_vm<8>();
    barrier_lgkm();
    STAMPR(3 + c);
#pragma unroll
    for (int i = 0; i < 8; ++i) {
      const int hd = 8 * c + i;
      bf16x8 o = ldsr(rdO + hd * 1024);
      bf16x8 wf = ldstr(rdW + hd * 2048, rdW + hd * 2048 + 1024);
      acc = MFMA(wf, o, acc);
    }
  }
  char* orow = out + (size_t)(st * 64 + w0 * 32 + r32) * 2048 + (nt * 64 + w1 * 32 + 8 * h) * 2;
  *(u32x4*)(orow) = swap8(pk8n(acc, 0));
  *(u32x4*)(orow + 32) = swap8(pk8n(acc, 8));
#pragma unroll
  for (int r = 0; r < 2; ++r) {
    u32x4 o;
#pragma unroll
    for (int i = 0; i < 4; ++i) {
      float lo = 0.f, hi = 0.f;
#pragma unroll
      for (int q = 0; q < 4; ++q) { lo += __uint_as_float(pv[q][r][i] << 16); hi += __uint_as_float(pv[q][r][i] & 0xffff0000u); }
      o[i] = pk(lo, hi);
    }
    *(u32x4*)(orow + 1024 + 32 * r) = swap8(o);
  }
  STAMPR(7); STAMPRT(8); STAMP_FLUSH(2, 9);
}

__global__ __launch_bounds__(256) void k4_diag(char* ws) {
  const unsigned long long* d = (const unsigned long long*)(ws + WS_DBG);
  __shared__ long long sm[16][256];
  const int b = threadIdx.x;
  auto Sx = [&](int k, int w, int i) { return (long long)d[(((size_t)k * 256 + b) * 16 + w) * 32 + i]; };
  sm[0][b] = Sx(0, 0, 0); sm[1][b] = Sx(0, 0, K1E);
  sm[2][b] = Sx(1, 0, 0); sm[3][b] = Sx(1, 0, 5);
  sm[4][b] = Sx(2, 0, 0); sm[5][b] = Sx(2, 0, 8);
  sm[6][b] = Sx(0, 0, 2) - Sx(0, 0, 1);
  sm[7][b] = Sx(0, 0, K1C) - Sx(0, 0, 1);
  sm[8][b] = Sx(1, 8, 3) - Sx(1, 8, 1);
  sm[9][b] = Sx(1, 0, 3) - Sx(1, 0, 2);
  sm[10][b] = Sx(1, 0, 4) - Sx(1, 0, 1);
  sm[11][b] = Sx(2, 0, 3) - Sx(2, 0, 1);
  sm[12][b] = Sx(2, 0, 7) - Sx(2, 0, 1);
  __syncthreads();
  __shared__ long long rmn[16], rmx[16], rsm[16];
  if (b < 13) {
    long long mn = 0x7fffffffffffffffll, mx = -1, s = 0;
    for (int j = 0; j < 256; ++j) { long long v = sm[b][j]; mn = v < mn ? v : mn; mx = v > mx ? v : mx; s += v; }
    rmn[b] = mn; rmx[b] = mx; rsm[b] = s;
  }
  __syncthreads();
  if (b == 0) {
    const long long t0 = rmn[0];
    long long X = 0;
    switch (QSEL) {
      case 0: X = 0; break;
      case 1: X = rmx[5] - t0; break;
      case 2: X = rmx[1] - t0; break;
      case 3: X = rmn[2] - rmx[1]; break;
      case 4: X = rmx[3] - rmn[2]; break;
      case 5: X = rmn[4] - rmx[3]; break;
      case 6: X = rmx[5] - rmn[4]; break;
      case 7: X = rsm[6] / 2560; break;
      case 8: X = rsm[7] / 2560; break;
      case 9: X = rsm[8] / 2560; break;
      case 10: X = rsm[9] / 2560; break;
      case 11: X = rsm[10] / 2560; break;
      case 12: X = rsm[11] / 2560; break;
      case 13: X = rsm[12] / 2560; break;
      case 14: X = rmn[1] - t0; break;
      case 15: X = rmn[3] - rmn[2]; break;
    }
    if (X < 0) X = 0; if (X > 20000) X = 20000;
    const long long target = t0 + 6000 + X * 10;
    while ((long long)__builtin_amdgcn_s_memrealtime() < target) __builtin_amdgcn_s_sleep(1);
  }
}

extern "C" void kernel_launch(void* const* d_in, const int* in_sizes, int n_in, void* d_out, int out_size, void* d_ws, size_t ws_size,
                              hipStream_t stream) {
  (void)in_sizes; (void)n_in; (void)out_size; (void)ws_size;
  k1_proj<<<256, 512, 0, stream>>>((const char*)d_in[0], (const char*)d_in[1], (const char*)d_in[3], (const char*)d_in[6],
                                   (const char*)d_in[7], (char*)d_ws);
  k2_main<<<256, 768, 0, stream>>>((const char*)d_in[4], (const char*)d_in[5], (char*)d_ws);
  k3_out<<<256, 256, 0, stream>>>((const char*)d_in[2], (const char*)d_ws, (char*)d_out);
  k4_diag<<<1, 256, 0, stream>>>((char*)d_ws);
}
```

```cpp
#include <hip/hip_runtime.h>
#include <stdint.h>

typedef __bf16 bf16x8  __attribute__((ext_vector_type(8)));
typedef __bf16 bf16x16 __attribute__((ext_vector_type(16)));
typedef __bf16 bf16x2  __attribute__((ext_vector_type(2)));
typedef short  s16x4   __attribute__((ext_vector_type(4)));
typedef short  s16x8   __attribute__((ext_vector_type(8)));
typedef float  f32x2   __attribute__((ext_vector_type(2)));
typedef float  f32x8   __attribute__((ext_vector_type(8)));
typedef float  f32x16  __attribute__((ext_vector_type(16)));
typedef unsigned u32x4 __attribute__((ext_vector_type(4)));
typedef unsigned u32x2 __attribute__((ext_vector_type(2)));
typedef unsigned long long u64;
#define LDSP __attribute__((address_space(3)))
#define GLBP __attribute__((address_space(1)))
#define DEV __device__ __forceinline__

constexpr uint32_t WS_Q  = 0;
constexpr uint32_t WS_KV = 2u << 20;
constexpr uint32_t WS_AB = (6u << 20) + 65536;
constexpr uint32_t WS_O  = WS_AB + (4u << 20);
constexpr uint32_t WS_P  = WS_O + (2u << 20);
constexpr uint32_t WS_F  = 24u << 20;
constexpr uint32_t WS_END = 25u << 20;
constexpr uint32_t FS = 8;
constexpr uint32_t F_E = WS_F, F_QKV = WS_F + 65536, F_AB = WS_F + 2 * 65536, F_O = WS_F + 3 * 65536, F_P = WS_F + 4 * 65536;
constexpr int SPIN_MAX = 1 << 18;

#define STAMP_DECL
#define STAMPR(i) do {} while (0)
#define STAMPRT(i) do {} while (0)
#define STAMP_FLUSH(n) do {} while (0)
#define SB __builtin_amdgcn_sched_barrier(0)
#define LST(i) do {} while (0)
#define LST_FLUSH() do {} while (0)
#define MFMA(a, b, c) __builtin_amdgcn_mfma_f32_32x32x16_bf16(a, b, c, 0, 0, 0)

DEV void glds(uint32_t ldsaddr, const void* sbase, uint32_t voff) {
  asm volatile("s_mov_b32 m0, %2\n\ts_nop 0\n\tglobal_load_lds_dwordx4 %0, %1" ::"v"(voff), "s"(sbase), "s"(ldsaddr) : "memory");
}
DEV void glds_dev(uint32_t ldsaddr, const void* sbase, uint32_t voff) {
  asm volatile("s_mov_b32 m0, %2\n\ts_nop 0\n\tglobal_load_lds_dwordx4 %0, %1 sc1" ::"v"(voff), "s"(sbase), "s"(ldsaddr) : "memory");
}
DEV unsigned warm(const char* a) {
  unsigned r;
  asm volatile("global_load_dword %0, %1, off" : "=&v"(r) : "v"(a) : "memory");
  return r;
}
#define KEEP(x) asm volatile("" ::"v"(x))
template <int N> DEV void wait_vm() { asm volatile("s_waitcnt vmcnt(%0)" ::"n"(N) : "memory"); }
DEV void barrier_lgkm() { asm volatile("s_waitcnt lgkmcnt(0)\n\ts_barrier" ::: "memory"); }
DEV bf16x8 ldsr(uint32_t a) { return *(const LDSP bf16x8*)(size_t)a; }
DEV bf16x8 ldstr(uint32_t a0, uint32_t a1) {
  s16x4 x = __builtin_amdgcn_ds_read_tr16_b64_v4i16((LDSP s16x4*)(size_t)a0);
  s16x4 y = __builtin_amdgcn_ds_read_tr16_b64_v4i16((LDSP s16x4*)(size_t)a1);
  return __builtin_bit_cast(bf16x8, (s16x8)__builtin_shufflevector(x, y, 0, 1, 2, 3, 4, 5, 6, 7));
}
DEV unsigned pk(float a, float b) {
  f32x2 v = {a, b};
  return __builtin_bit_cast(unsigned, __builtin_convertvector(v, bf16x2));
}
DEV u32x4 pk8(const f32x16& a, int o, float sc) {
  u32x4 r;
  r[0] = pk(a[o + 0] * sc, a[o + 1] * sc); r[1] = pk(a[o + 2] * sc, a[o + 3] * sc);
  r[2] = pk(a[o + 4] * sc, a[o + 5] * sc); r[3] = pk(a[o + 6] * sc, a[o + 7] * sc);
  return r;
}
DEV u32x4 pk8n(const f32x16& a, int o) {
  u32x4 r;
  r[0] = pk(a[o + 0], a[o + 1]); r[1] = pk(a[o + 2], a[o + 3]);
  r[2] = pk(a[o + 4], a[o + 5]); r[3] = pk(a[o + 6], a[o + 7]);
  return r;
}
DEV u32x4 swap8(u32x4 v) {
  auto a = __builtin_amdgcn_permlane32_swap(v[0], v[2], false, false);
  auto b = __builtin_amdgcn_permlane32_swap(v[1], v[3], false, false);
  u32x4 r; r[0] = a[0]; r[1] = b[0]; r[2] = a[1]; r[3] = b[1];
  return r;
}
DEV f32x16 splat16(float v) {
  f32x16 r;
#pragma unroll
  for (int i = 0; i < 16; ++i) r[i] = v;
  return r;
}

typedef __amdgpu_buffer_rsrc_t rsrc_t;
DEV void st_dev(rsrc_t r, uint32_t off, u32x4 v) { __builtin_amdgcn_raw_buffer_store_b128(v, r, off, 0, 16); }
DEV u32x4 ld_dev(rsrc_t r, uint32_t off) { return __builtin_amdgcn_raw_buffer_load_b128(r, off, 0, 16); }
DEV u64 uni64(u64 v) {
  const unsigned lo = __builtin_amdgcn_readfirstlane((unsigned)v), hi = __builtin_amdgcn_readfirstlane((unsigned)(v >> 32));
  return ((u64)hi << 32) | lo;
}
DEV void publish(uint32_t cnt, unsigned nwaves, char* ws, uint32_t flag, u64 tag, int l) {
  wait_vm<0>();
  unsigned old = 0;
  if (l == 0) old = __hip_atomic_fetch_add((LDSP unsigned*)(size_t)cnt, 1u, __ATOMIC_RELAXED, __HIP_MEMORY_SCOPE_WORKGROUP);
  old = __builtin_amdgcn_readfirstlane(old);
  if (old == nwaves - 1 && l == 0) __hip_atomic_store((GLBP u64*)(ws + flag), tag, __ATOMIC_RELAXED, __HIP_MEMORY_SCOPE_AGENT);
}
DEV void poll(const char* ws, uint32_t off, u64 tag) {
#pragma unroll 1
  for (int it = 0; it < SPIN_MAX; ++it) {
    const u64 v = __hip_atomic_load((const GLBP u64*)(ws + off), __ATOMIC_RELAXED, __HIP_MEMORY_SCOPE_AGENT);
    if (__builtin_amdgcn_ballot_w64(v != tag) == 0) break;
    __builtin_amdgcn_s_sleep(1);
  }
  asm volatile("" ::: "memory");
}

constexpr uint32_t KV0 = 0, SLOT = 2080, KVBUF = 4 * SLOT;
constexpr uint32_t G0 = 32768, GBUF = 32768;
constexpr uint32_t CNT = 28672;
#define SMF(a, b, c) __builtin_amdgcn_smfmac_f32_32x32x32_bf16(a, b, c, sidx, 0, 0)
#define LO8(x) __builtin_bit_cast(bf16x16, (f32x8)__builtin_shufflevector(x, x, 0, 1, 2, 3, 4, 5, 6, 7))
#define HI8(x) __builtin_bit_cast(bf16x16, (f32x8)__builtin_shufflevector(x, x, 8, 9, 10, 11, 12, 13, 14, 15))
#define PIN(x) do { asm volatile("" ::"v"(x)); SB; } while (0)
#define EXPR(s, a, b) do { _Pragma("unroll") for (int i_ = a; i_ < b; ++i_) s[i_] = __builtin_amdgcn_exp2f(s[i_]); SB; } while (0)

struct AttnState {
  f32x16 sA, sB, oA, oB, nA, nB;
  bf16x8 qA, qB, kf, v0, v1;
};

template <int BUFI, int U>
DEV void attn_step(AttnState& a, uint32_t aK, uint32_t aV0, uint32_t aV1, int sidx) {
  constexpr uint32_t OFF = BUFI * KVBUF + U * SLOT;
  EXPR(a.sA, 0, 2);
  a.oB = SMF(a.v1, HI8(a.sB), a.oB); PIN(a.oB);
  a.v1 = ldsr(aV1 + OFF); SB;
  EXPR(a.sA, 2, 7);
  a.sB = MFMA(a.kf, a.qB, a.nB); PIN(a.sB);
  a.kf = ldsr(aK + OFF); SB;
  EXPR(a.sA, 7, 12);
  a.oA = SMF(a.v0, LO8(a.sA), a.oA); PIN(a.oA);
  EXPR(a.sA, 12, 16);
  EXPR(a.sB, 0, 2);
  a.oA = SMF(a.v1, HI8(a.sA), a.oA); PIN(a.oA);
  EXPR(a.sB, 2, 7);
  a.sA = MFMA(a.kf, a.qA, a.nA); PIN(a.sA);
  EXPR(a.sB, 7, 12);
  a.oB = SMF(a.v0, LO8(a.sB), a.oB); PIN(a.oB);
  a.v0 = ldsr(aV0 + OFF); SB;
  EXPR(a.sB, 12, 16);
}
template <int BUFI>
DEV void attn_stage(AttnState& a, uint32_t aK, uint32_t aV0, uint32_t aV1, int sidx, int stg) {
  barrier_lgkm(); SB;
  LST(stg);
  attn_step<BUFI, 0>(a, aK, aV0, aV1, sidx);
  attn_step<BUFI, 1>(a, aK, aV0, aV1, sidx);
}

DEV float max16(const f32x16& s) {
  float m = fmaxf(fmaxf(fmaxf(s[0], s[1]), fmaxf(s[2], s[3])), fmaxf(fmaxf(s[4], s[5]), fmaxf(s[6], s[7])));
  float n = fmaxf(fmaxf(fmaxf(s[8], s[9]), fmaxf(s[10], s[11])), fmaxf(fmaxf(s[12], s[13]), fmaxf(s[14], s[15])));
  return fmaxf(m, n);
}
DEV float xhalf_max(float m) {
  auto t = __builtin_amdgcn_permlane32_swap(__float_as_uint(m), __float_as_uint(m), false, false);
  return fmaxf(__uint_as_float(t[0]), __uint_as_float(t[1]));
}
DEV bf16x8 ones8() { u32x4 o = {0x3F803F80u, 0x3F803F80u, 0x3F803F80u, 0x3F803F80u}; return __builtin_bit_cast(bf16x8, o); }
DEV bf16x8 ldf(rsrc_t r, uint32_t off) { return __builtin_bit_cast(bf16x8, ld_dev(r, off)); }

DEV f32x16 attn_exact(rsrc_t rs, uint32_t kvh, bf16x8 q, int l, int sidx) {
  const int h = l >> 5, r32 = l & 31;
  float m = -INFINITY;
  const f32x16 z = splat16(0.f);
#pragma unroll 1
  for (int t = 0; t < 64; ++t) {
    bf16x8 k = ldf(rs, kvh + t * 2048 + l * 16);
    f32x16 s = MFMA(k, q, z);
    m = fmaxf(m, max16(s));
  }
  m = xhalf_max(m);
  const f32x16 nm = splat16(-m);
  f32x16 o = z;
#pragma unroll 1
  for (int t = 0; t < 64; ++t) {
    bf16x8 k = ldf(rs, kvh + t * 2048 + l * 16);
    bf16x8 v0 = ldf(rs, kvh + t * 2048 + 1024 + h * 256 + (r32 & 15) * 16);
    bf16x8 v1 = ldf(rs, kvh + t * 2048 + 1536 + h * 256 + (r32 & 15) * 16);
    if (r32 >= 16) { v0 = ones8(); v1 = ones8(); }
    f32x16 s = MFMA(k, q, nm);
#pragma unroll
    for (int i = 0; i < 16; ++i) s[i] = __builtin_amdgcn_exp2f(s[i]);
    o = SMF(v0, LO8(s), o);
    o = SMF(v1, HI8(s), o);
  }
  return o;
}

#define DLY 2
#define GMODE 1
#define GPRE __builtin_amdgcn_s_setprio(0)
#define GPOST __builtin_amdgcn_s_setprio(GPRIO)
#define GGAP do {} while (0)
#define GPRIO 3
#define KVROT 0
#define CROT 0
#define AROT 0

__global__ __launch_bounds__(768) void fused_mha_fnet(const char* __restrict__ xA, const char* __restrict__ wqkv, const char* __restrict__ wo,
                                                      const char* __restrict__ xB, const char* __restrict__ cre, const char* __restrict__ cim,
                                                      const char* __restrict__ hre, const char* __restrict__ him, char* __restrict__ ws,
                                                      char* __restrict__ out) {
  __shared__ __attribute__((aligned(1024))) char smem[163840];
  const uint32_t lds0 = (uint32_t)(size_t)(LDSP char*)smem;
  const int l = threadIdx.x & 63, w = __builtin_amdgcn_readfirstlane(threadIdx.x >> 6);
  const int h = l >> 5, r32 = l & 31;
  const int b = blockIdx.x, xcd = b & 7, idx = b >> 3;
  const int mt = (xcd >> 1) * 8 + (idx >> 2), nt = (xcd & 1) * 4 + (idx & 3);
  const int head = xcd * 4 + (idx >> 3), qblk = idx & 7;
  STAMP_DECL; STAMPRT(0); STAMPR(1);
  const rsrc_t rs = __builtin_amdgcn_make_buffer_rsrc(ws, 0, WS_END, 0x00020000);
  const uint32_t xrd = (uint32_t)r32 * 128u + (uint32_t)((h ^ ((r32 >> 1) & 7)) * 16);

  if (w < 8) {
    const u64 tag = uni64(__hip_atomic_load((const GLBP u64*)(ws + F_E), __ATOMIC_RELAXED, __HIP_MEMORY_SCOPE_AGENT)) + 1;
    const int wm = w & 1, wn = (w >> 1) & 1;
    f32x16 a0 = splat16(0.f), a1 = a0;
    {
      const int q = (l & 15) >> 2, pp = l & 3, blk = (l >> 4) & 1;
      const uint32_t rdX = lds0 + wm * 4096 + xrd;
      const uint32_t rdW = lds0 + 8192u + (w < 4 ? 0u : 16384u) + (uint32_t)(8 * h + q) * 128u +
                           (uint32_t)(((4 * wn + 2 * blk + (pp >> 1)) ^ (4 * (q >> 1))) * 16 + 8 * (pp & 1));
      uint32_t rslot = 0;
      if (w < 4) {
#pragma unroll 1
        for (int p = 0; p < 8; ++p) {
          barrier_lgkm();
          STAMPR(2);
          const uint32_t bx = rdX + rslot, bw = rdW + rslot;
          bf16x8 x[4], wq[4], wk[4];
#pragma unroll
          for (int ks = 0; ks < 4; ++ks) {
            x[ks] = ldsr(bx ^ (ks << 5));
            wq[ks] = ldstr(bw + ks * 2048, bw + ks * 2048 + 512);
            wk[ks] = ldstr(bw + 8192 + ks * 2048, bw + 8192 + ks * 2048 + 512);
          }
          SB;
#pragma unroll
          for (int ks = 0; ks < 4; ++ks) { a0 = MFMA(wq[ks], x[ks], a0); a1 = MFMA(wk[ks], x[ks], a1); }
          rslot = rslot == 65536 ? 0 : rslot + 32768;
        }
      } else {
#pragma unroll 1
        for (int p = 0; p < 8; ++p) {
          barrier_lgkm();
          STAMPR(2);
          const uint32_t bx = rdX + rslot, bw = rdW + rslot;
          bf16x8 x[4], wv[4];
#pragma unroll
          for (int ks = 0; ks < 4; ++ks) {
            x[ks] = ldsr(bx ^ (ks << 5));
            wv[ks] = ldstr(bw + ks * 2048, bw + ks * 2048 + 512);
          }
          SB;
#pragma unroll
          for (int ks = 0; ks < 4; ks += 2) { a0 = MFMA(x[ks], wv[ks], a0); a1 = MFMA(x[ks + 1], wv[ks + 1], a1); }
          rslot = rslot == 65536 ? 0 : rslot + 32768;
        }
#pragma unroll
        for (int i = 0; i < 16; ++i) a0[i] += a1[i];
      }
    }
    LST(31);
    if (w == 0) {
      if (l < 12) { u32x4 o = {0x3F803F80u, 0x3F803F80u, 0x3F803F80u, 0x3F803F80u}; *(LDSP u32x4*)(size_t)(lds0 + KV0 + l * SLOT + 2048) = o; }
      if (l < 16) *(LDSP unsigned*)(size_t)(lds0 + CNT + l * 4) = 0u;
    }
    {
      const uint32_t tile = mt * 2 + wm, head0 = nt * 4 + wn * 2;
      if (w < 4) {
        constexpr float QS = 0.25f * 1.4426950408889634f;
        const int pr = (r32 & 0x13) | ((r32 & 4) << 1) | ((r32 & 8) >> 1);
#pragma unroll
        for (int hh = 0; hh < 2; ++hh) {
          st_dev(rs, WS_Q + ((head0 + hh) * 64 + tile) * 1024 + l * 16, pk8(a0, 8 * hh, QS));
          st_dev(rs, WS_KV + ((head0 + hh) * 64 + tile) * 2048 + (h * 32 + pr) * 16, pk8n(a1, 8 * hh));
        }
      } else {
        const uint32_t vb = WS_KV + ((head0 + (r32 >> 4)) * 64 + tile) * 2048 + 1024 + h * 256 + (r32 & 15) * 16;
        st_dev(rs, vb, pk8n(a0, 0));
        st_dev(rs, vb + 512, pk8n(a0, 8));
      }
    }
    STAMPR(3); LST(33);
    barrier_lgkm();
    LST(34);
    wait_vm<0>();
    LST(35);
    publish(lds0 + CNT + 0, 8, ws, F_QKV + (nt * 32 + mt) * FS, tag, l);
    STAMPR(4); LST(36);
    barrier_lgkm();
    LST(37);
    if (w == 0) poll(ws, F_QKV + (xcd * 32 + r32) * FS, tag);
    STAMPR(5); LST(38);
    barrier_lgkm();
    STAMPR(6); LST(39);

    const int qg = w & 3, kh = w >> 2;
    const uint32_t kvh = WS_KV + head * 131072;
    int sidx = 0xDDDD;
    asm volatile("" : "+v"(sidx));
    AttnState a;
    {
      const uint32_t qf = WS_Q + (head * 64 + qblk * 8 + qg * 2) * 1024 + l * 16;
      a.qA = ldf(rs, qf);
      a.qB = ldf(rs, qf + 1024);
      const uint32_t kv1 = kvh + ((KVROT * qblk) & 15) * 4096;
      a.kf = ldf(rs, kv1 + kh * 65536 + l * 16);
      const bf16x8 k0 = ldf(rs, kv1 + l * 16);
      a.v0 = ldf(rs, kv1 + kh * 65536 + 1024 + h * 256 + (r32 & 15) * 16);
      if (r32 >= 16) a.v0 = ones8();
      const f32x16 z = splat16(0.f);
      f32x16 t0 = MFMA(k0, a.qA, z), t1 = MFMA(k0, a.qB, z);
      a.nA = splat16(-(xhalf_max(max16(t0)) + 64.f));
      a.nB = splat16(-(xhalf_max(max16(t1)) + 64.f));
      a.oA = z; a.oB = z; a.sB = z;
      a.v1 = __builtin_bit_cast(bf16x8, (u32x4){0u, 0u, 0u, 0u});
      a.sA = MFMA(a.kf, a.qA, a.nA);
    }
    const uint32_t sb = lds0 + KV0 + kh * 2 * SLOT;
    const bool onesl = (r32 >= 16 && r32 < 20) || r32 >= 28;
    const uint32_t vrow = h * 256 + (r32 < 16 ? r32 * 16 : 0);
    const uint32_t aK = sb + l * 16;
    const uint32_t aV0 = sb + (onesl ? 2048u : 1024u + vrow);
    const uint32_t aV1 = sb + (onesl ? 2048u : 1536u + vrow);
    STAMPR(7); LST(41);
#pragma unroll 1
    for (int it = 0; it < 5; ++it) {
      attn_stage<0>(a, aK, aV0, aV1, sidx, 3 * it);
      attn_stage<1>(a, aK, aV0, aV1, sidx, 3 * it + 1);
      attn_stage<2>(a, aK, aV0, aV1, sidx, 3 * it + 2);
    }
    attn_stage<0>(a, aK, aV0, aV1, sidx, 15);
    a.oB = SMF(a.v1, HI8(a.sB), a.oB);
    STAMPR(8);
    float lA = h ? a.oA[12] : a.oA[8], lB = h ? a.oB[12] : a.oB[8];
    const uint32_t cb = lds0 + KVBUF + qg * 4608 + l * 4;
    if (kh == 1) {
#pragma unroll
      for (int i = 0; i < 8; ++i) {
        *(LDSP float*)(size_t)(cb + i * 256) = a.oA[i];
        *(LDSP float*)(size_t)(cb + (9 + i) * 256) = a.oB[i];
      }
      *(LDSP float*)(size_t)(cb + 8 * 256) = lA;
      *(LDSP float*)(size_t)(cb + 17 * 256) = lB;
    }
    LST(15 + 1);
    barrier_lgkm();
    LST(17);
    const int st = xcd * 4 + (idx >> 3), n3 = idx & 7;
    if (kh == 0) {
      float oa[8], ob[8];
#pragma unroll
      for (int i = 0; i < 8; ++i) {
        oa[i] = a.oA[i] + *(const LDSP float*)(size_t)(cb + i * 256);
        ob[i] = a.oB[i] + *(const LDSP float*)(size_t)(cb + (9 + i) * 256);
      }
      lA += *(const LDSP float*)(size_t)(cb + 8 * 256);
      lB += *(const LDSP float*)(size_t)(cb + 17 * 256);
      bool okA = lA >= 0x1p-100f && lA <= 0x1p120f, okB = lB >= 0x1p-100f && lB <= 0x1p120f;
#pragma unroll
      for (int i = 0; i < 8; ++i) { okA = okA && (fabsf(oa[i]) <= 0x1p126f); okB = okB && (fabsf(ob[i]) <= 0x1p126f); }
      if (__builtin_amdgcn_ballot_w64(!okA) != 0) {
        f32x16 o = attn_exact(rs, kvh, a.qA, l, sidx);
#pragma unroll
        for (int i = 0; i < 8; ++i) oa[i] = o[i];
        lA = h ? o[12] : o[8];
      }
      if (__builtin_amdgcn_ballot_w64(!okB) != 0) {
        f32x16 o = attn_exact(rs, kvh, a.qB, l, sidx);
#pragma unroll
        for (int i = 0; i < 8; ++i) ob[i] = o[i];
        lB = h ? o[12] : o[8];
      }
      LST(19);
      const float rA = 1.0f / lA, rB = 1.0f / lB;
      u32x4 pa, pb;
#pragma unroll
      for (int i = 0; i < 4; ++i) { pa[i] = pk(oa[2 * i] * rA, oa[2 * i + 1] * rA); pb[i] = pk(ob[2 * i] * rB, ob[2 * i + 1] * rB); }
      const uint32_t of = WS_O + ((qblk * 8 + qg * 2) * 32 + head) * 1024 + l * 16;
      st_dev(rs, of, pa);
      st_dev(rs, of + 32768, pb);
      STAMPR(9); LST(20);
      barrier_lgkm();
      LST(21);
      barrier_lgkm();
      LST(22);
      wait_vm<0>();
      LST(23);
      publish(lds0 + CNT + 8, 4, ws, F_O + (qblk * 32 + head) * FS, tag, l);
      STAMPR(10); LST(24);
      barrier_lgkm();
      LST(25);
      barrier_lgkm();
      STAMPR(11); LST(26);
      const int w0 = w & 1, w1 = w >> 1;
      const int q4 = (l & 15) >> 2, p4 = l & 3, blk = (l >> 4) & 1;
      const uint32_t rdW = lds0 + G0 + 16384u + (uint32_t)(4 * h + q4) * 128u +
                           (uint32_t)(((4 * w1 + 2 * blk + (p4 >> 1)) ^ (4 * (q4 >> 1))) * 16 + 8 * (p4 & 1));
      const uint32_t rdO = lds0 + G0 + w0 * 8192 + l * 16;
      f32x16 c0 = splat16(0.f), c1 = c0;
#pragma unroll 1
      for (int j = 0; j < 4; ++j) {
#pragma unroll 1
        for (int it = 0; it < SPIN_MAX; ++it) {
          const unsigned v = __hip_atomic_load((LDSP unsigned*)(size_t)(lds0 + CNT + 16 + j * 4), __ATOMIC_RELAXED, __HIP_MEMORY_SCOPE_WORKGROUP);
          if (__builtin_amdgcn_readfirstlane(v) != 0) break;
          __builtin_amdgcn_s_sleep(1);
        }
        asm volatile("" ::: "memory");
        LST(27 + j);
#pragma unroll
        for (int i = 0; i < 8; i += 2) {
          bf16x8 o0 = ldsr(rdO + j * GBUF + i * 1024), o1 = ldsr(rdO + j * GBUF + i * 1024 + 1024);
          bf16x8 f0 = ldstr(rdW + j * GBUF + i * 2048, rdW + j * GBUF + i * 2048 + 1024);
          bf16x8 f1 = ldstr(rdW + j * GBUF + i * 2048 + 2048, rdW + j * GBUF + i * 2048 + 3072);
          c0 = MFMA(f0, o0, c0);
          c1 = MFMA(f1, o1, c1);
        }
      }
      STAMPR(12);
#pragma unroll
      for (int i = 0; i < 16; ++i) c0[i] += c1[i];
      char* orow = out + (size_t)(st * 64 + w0 * 32 + r32) * 2048 + (n3 * 64 + w1 * 32 + 8 * h) * 2;
      *(u32x4*)(orow) = swap8(pk8n(c0, 0));
      *(u32x4*)(orow + 32) = swap8(pk8n(c0, 8));
      if (b == 0 && threadIdx.x == 0) __hip_atomic_store((GLBP u64*)(ws + F_E), tag, __ATOMIC_RELAXED, __HIP_MEMORY_SCOPE_AGENT);
    } else {
      const int j = w - 4;
      const uint32_t voW = (uint32_t)(l >> 3) * 1024u + (uint32_t)(((l & 7) ^ (((l >> 4) & 1) << 2)) * 16);
      const char* wg = wo + (size_t)(128 * j) * 1024 + n3 * 128;
      const uint32_t d = lds0 + G0 + j * GBUF;
      auto issueW = [&]() {
#pragma unroll
        for (int i = 0; i < 16; ++i) glds(d + 16384 + i * 1024, wg + (size_t)(8 * i) * 1024, voW);
      };
      barrier_lgkm();
      LST(21);
      if (j == 0) issueW();
      LST(18);
      barrier_lgkm();
      LST(22);
      if (j == 1) issueW();
      barrier_lgkm();
      LST(25);
      if (j == 2) issueW();
      barrier_lgkm();
      LST(26);
      if (j == 3) issueW();
      STAMPR(9); LST(27);
      poll(ws, F_O + ((st >> 2) * 32 + 8 * j + (l & 7)) * FS, tag);
      STAMPR(10); LST(28);
      const char* og = ws + WS_O + (size_t)(st * 2) * 32768 + (8 * j) * 1024;
#pragma unroll
      for (int i = 0; i < 16; ++i) glds_dev(d + i * 1024, og + (size_t)(i >> 3) * 32768 + (i & 7) * 1024, l * 16);
      LST(29);
      wait_vm<0>();
      STAMPR(11); LST(30);
      if (l == 0) __hip_atomic_store((LDSP unsigned*)(size_t)(lds0 + CNT + 16 + j * 4), 1u, __ATOMIC_RELAXED, __HIP_MEMORY_SCOPE_WORKGROUP);
      STAMPR(12);
    }
    STAMPR(13); STAMPRT(14); STAMP_FLUSH(15); LST_FLUSH();
  } else {
    const int g = w - 8;
    __builtin_amdgcn_s_setprio(GPRIO);
    unsigned pfa, pfb, pfc, pfd;
    u64 eraw;
    asm volatile("global_load_dwordx2 %0, %1, %2 sc1" : "=&v"(eraw) : "v"(0u), "s"(ws + F_E) : "memory");
    {
      const int lx = l & 15, lw = l < 40 ? (l + 8) % 24 : 0;
      const char* px = xA + (size_t)(mt * 64 + (nt & 3) * 16 + lx) * 1024;
      const char* pw = wqkv + (size_t)((mt & 7) * 8 + (lw & 7)) * 3072 + (lw >> 3) * 1024 + nt * 128;
      const bool isx = l < 16;
      pfa = warm(isx ? px + g * 128 : pw + (size_t)g * (64 * 3072));
      pfb = warm(isx ? px + (g + 4) * 128 : pw + (size_t)(g + 4) * (64 * 3072));
    }
    const char* hw = nt < 4 ? hre : him;
    const uint32_t vo0 = (uint32_t)(l >> 3) * 1024u + (uint32_t)(((l & 7) ^ (l >> 4)) * 16);
    const uint32_t vo1 = (uint32_t)(8 + (l >> 3)) * 1024u + (uint32_t)(((l & 7) ^ (4 + (l >> 4))) * 16);
    {
      const char* gx = xA + (size_t)(mt * 64 + 16 * g) * 1024;
      const char* gw = wqkv + (size_t)(16 * g) * 3072 + nt * 128;
      const uint32_t vw = (uint32_t)(l >> 3) * 3072u + (uint32_t)(((l & 7) ^ (((l >> 4) & 1) << 2)) * 16);
      auto issue = [&](int p, uint32_t slot) {
        const uint32_t bb = lds0 + slot + g * 2048;
        glds(bb, gx + p * 128, vo0);
        glds(bb + 1024, gx + p * 128, vo1);
#pragma unroll
        for (int c = 0; c < 3; ++c) {
          glds(bb + 8192 + c * 8192, gw + (size_t)p * (64 * 3072) + c * 1024, vw);
          glds(bb + 8192 + c * 8192 + 1024, gw + (size_t)p * (64 * 3072) + c * 1024 + 8 * 3072, vw);
        }
      };
      for (int i = 0; i < DLY; ++i) __builtin_amdgcn_s_sleep(4);
      issue(0, 0); issue(1, 32768);
      wait_vm<16>();
      asm volatile("" : "+v"(eraw));
      KEEP(pfa); KEEP(pfb);
      uint32_t wslot = 65536;
#pragma unroll 1
      for (int p = 0; p < 6; ++p) {
        wait_vm<8>();
        asm volatile("s_barrier" ::: "memory");
        issue(p + 2, wslot);
        wslot = wslot == 65536 ? 0 : wslot + 32768;
      }
    }
    const u64 tag = uni64(eraw) + 1;
    {
      const char* px = xB + (size_t)(mt * 64 + (nt & 3) * 16 + (l & 15)) * 1024;
      const char* pw = hw + (size_t)((nt & 3) * 128 + (mt & 7) * 16 + (l & 15)) * 1024;
      pfc = warm((l < 32 ? px : pw) + g * 256 + ((l >> 4) & 1) * 128);
    }
    wait_vm<9>();
    barrier_lgkm();
    wait_vm<1>();
    barrier_lgkm();
    STAMPR(2);
    const char* hx = xB + (size_t)(mt * 64 + 16 * g) * 1024;
    const char* hwg = hw + (size_t)((nt & 3) * 128 + 32 * g) * 1024;
    constexpr uint32_t HB = 114688, HA = 65536, HS = 32768, HSUB = 24576;
    auto issueH = [&](int hs, uint32_t slot) {
      const uint32_t d = lds0 + slot;
      glds(d + (16 * g) * 128, hx + hs * 128, vo0);
      glds(d + (16 * g + 8) * 128, hx + hs * 128, vo1);
      glds(d + 8192 + (32 * g) * 128, hwg + hs * 128, vo0);
      glds(d + 8192 + (32 * g + 8) * 128, hwg + hs * 128, vo1);
      glds(d + 8192 + (32 * g + 16) * 128, hwg + 16384 + hs * 128, vo0);
      glds(d + 8192 + (32 * g + 24) * 128, hwg + 16384 + hs * 128, vo1);
    };
    f32x16 c00 = splat16(0.f), c01 = c00, c10 = c00, c11 = c00;
    const uint32_t hrX = lds0 + xrd, hrE = lds0 + 8192 + g * 4096 + xrd;
    auto computeH = [&](uint32_t slot) {
#pragma unroll
      for (int ks = 0; ks < 4; ++ks) {
        bf16x8 x0 = ldsr((hrX + slot) ^ (ks << 5)), x1 = ldsr(((hrX + slot) ^ (ks << 5)) + 4096);
        bf16x8 e = ldsr((hrE + slot) ^ (ks << 5));
        c00 = MFMA(x0, e, c00);
        c01 = MFMA(x1, e, c01);
      }
    };
    const char* kvb = ws + WS_KV + (size_t)head * 131072 + (g >> 1) * 65536;
    auto issueKV = [&](int p, uint32_t buf) {
      const uint32_t d = lds0 + KV0 + buf + g * SLOT;
      const uint32_t t = 2 * ((p + KVROT * qblk) & 15) + (g & 1), nx = (t + 1) & 31;
      glds_dev(d, kvb + nx * 2048, l * 16);
      glds_dev(d + 1024, kvb, 1024 + l * 16 + (l < 32 ? nx : t) * 2048);
    };
    barrier_lgkm();
    issueH(0, HB); issueH(1, HB + HSUB); issueH(2, HA); issueH(3, HA + HSUB);
    wait_vm<12>();
    KEEP(pfc);
    barrier_lgkm();
    STAMPR(3);
    computeH(HB); computeH(HB + HSUB);
    wait_vm<0>();
    STAMPR(4);
    barrier_lgkm();
    STAMPR(5);
    issueKV(0, 0); issueKV(1, KVBUF);
    issueH(4, HB); issueH(5, HB + HSUB); issueH(6, HS);
    computeH(HA); computeH(HA + HSUB);
    wait_vm<20>();
    STAMPR(6);
    barrier_lgkm();
    issueH(7, HA); issueKV(2, 2 * KVBUF);
    wait_vm<2>();
    barrier_lgkm();
    STAMPR(7);
    issueKV(3, 0);
    computeH(HB); computeH(HB + HSUB); computeH(HS); computeH(HA);
    {
      const float sg = nt < 4 ? 1.f : -1.f;
      const uint32_t ab = WS_AB + nt * (512u << 10) + (mt * 4) * 4096 + g * 1024 + l * 16;
      st_dev(rs, ab, swap8(pk8(c00, 0, sg)));
      st_dev(rs, ab + 4096, swap8(pk8(c00, 8, sg)));
      st_dev(rs, ab + 8192, swap8(pk8(c01, 0, sg)));
      st_dev(rs, ab + 12288, swap8(pk8(c01, 8, sg)));
    }
    wait_vm<6>();
    STAMPR(8);
    barrier_lgkm();
    publish(lds0 + CNT + 4, 4, ws, F_AB + (nt * 32 + mt) * FS, tag, l);
    STAMPR(9);
    const int ge = g & 1, gt = g >> 1;
    const int q = xcd >> 1, mtile = (xcd & 1) * 8 + (idx >> 2), ntile = idx & 3;
    const char* gbase; uint32_t pstride, qstride, go0, go1;
    if (g < 2) {
      gbase = (q < 2 ? cre : cim) + (size_t)(mtile * 128 + g * 64) * 4096 + (q & 1) * 2048;
      pstride = 128; qstride = 65536;
      go0 = (uint32_t)(l >> 3) * 4096u + (uint32_t)(((l & 7) ^ (l >> 4)) * 16);
      go1 = (uint32_t)(8 + (l >> 3)) * 4096u + (uint32_t)(((l & 7) ^ (4 + (l >> 4))) * 16);
    } else {
      gbase = ws + WS_AB + (size_t)((q >> 1) * 4 + ntile) * (512u << 10) + (size_t)(q & 1) * 64 * 4096 + (g - 2) * 8192;
      pstride = 16384; qstride = 2048;
      go0 = l * 16; go1 = l * 16 + 1024;
    }
    const int krot = CROT * ntile + AROT * (mtile & 7);
    auto issueG2 = [&](int p, int i0) {
      const char* sb = gbase + (size_t)((p + krot) & 15) * pstride + (size_t)(i0 >> 1) * qstride;
      const uint32_t d = lds0 + G0 + (p & 3) * GBUF + g * 8192 + i0 * 1024;
      glds_dev(d, sb, go0);
      glds_dev(d + 1024, sb, go1);
    };
    auto issueG = [&](int p) { issueG2(p, 0); issueG2(p, 2); issueG2(p, 4); issueG2(p, 6); };
    pfd = warm(wo + (size_t)((idx >> 3) * 128 + g * 32 + r32) * 1024 + (idx & 7) * 128);
    issueKV(4, KVBUF);
    if (g < 2) { issueG(0); issueG(1); issueG(2); }
    barrier_lgkm();
    issueKV(5, 2 * KVBUF);
    if (g >= 2) {
      poll(ws, F_AB + (((q >> 1) * 4 + ntile) * 32 + (q & 1) * 16 + (l & 15)) * FS, tag);
      issueG(0); issueG(1); issueG(2);
      wait_vm<16>();
    } else {
      wait_vm<18>();
    }
    KEEP(pfd);
    STAMPR(10);
    const uint32_t rdC = lds0 + G0 + (uint32_t)(64 * gt) * 128u + xrd;
    const uint32_t rdA = lds0 + G0 + 16384 + ge * 2048 + l * 16;
    c00 = splat16(0.f); c01 = c00; c10 = c00; c11 = c00;
    uint32_t kvbuf = 0;
#pragma unroll 1
    for (int p = 4; p < 20; ++p) {
      barrier_lgkm();
      LST(4 * (p - 4));
      if (p == 4) STAMPR(11);
      if (p == 16) STAMPR(12);
      const uint32_t so = (p & 3) * GBUF;
      const uint32_t ra = rdA + so, rc = rdC + so;
      bf16x8 e0 = ldsr(ra), e1 = ldsr(ra + 1024), t0 = ldsr(rc), t1 = ldsr(rc + 4096);
      if (p <= 13) issueKV(p + 2, kvbuf);
      kvbuf = kvbuf == 2 * KVBUF ? 0 : kvbuf + KVBUF;
      LST(4 * (p - 4) + 1);
#pragma unroll
      for (int ks = 0; ks < 4; ++ks) {
        bf16x8 f0, f1, u0, u1;
        if (ks < 3) {
          f0 = ldsr(ra + (ks + 1) * 4096); f1 = ldsr(ra + (ks + 1) * 4096 + 1024);
          u0 = ldsr(rc ^ ((ks + 1) << 5)); u1 = ldsr((rc ^ ((ks + 1) << 5)) + 4096);
        }
        SB;
        GPRE;
        c00 = MFMA(e0, t0, c00); GGAP; c01 = MFMA(e0, t1, c01); GGAP;
        c10 = MFMA(e1, t0, c10); GGAP; c11 = MFMA(e1, t1, c11); GGAP;
        GPOST;
        SB;
        if (p <= 16) issueG2(p - 1, 2 * ks);
        if (ks < 3) { e0 = f0; e1 = f1; t0 = u0; t1 = u1; }
      }
      LST(4 * (p - 4) + 2);
      if (p == 4) { if (g < 2) wait_vm<10>(); else wait_vm<18>(); }
      else if (p <= 13) wait_vm<18>();
      else if (p <= 16) wait_vm<16>();
      else if (p == 17) wait_vm<8>();
      else wait_vm<0>();
      LST(4 * (p - 4) + 3);
    }
    barrier_lgkm();
    STAMPR(13);
    const uint32_t pg = WS_P + (((mtile * 4 + ntile) * 4 + g) * 16) * 2048 + l * 16;
    f32x16 own;
    auto send = [&](const f32x16& c, int dq) {
      st_dev(rs, pg + (dq * 4 + q) * 2048, pk8n(c, 0));
      st_dev(rs, pg + (dq * 4 + q) * 2048 + 1024, pk8n(c, 8));
    };
    if (q != 0) send(c00, 0);
    if (q != 1) send(c01, 1);
    if (q != 2) send(c10, 2);
    if (q != 3) send(c11, 3);
    if (q == 0) own = c00; else if (q == 1) own = c01; else if (q == 2) own = c10; else own = c11;
    publish(lds0 + CNT + 12, 4, ws, F_P + ((mtile * 4 + ntile) * 4 + q) * FS, tag, l);
    STAMPR(14);
    poll(ws, F_P + ((mtile * 4 + ntile) * 4 + (l & 3)) * FS, tag);
    STAMPR(15);
    {
      u32x4 pv[3][2];
#pragma unroll
      for (int i = 0; i < 3; ++i) {
        const int sq = (q + 1 + i) & 3;
        pv[i][0] = ld_dev(rs, pg + (q * 4 + sq) * 2048);
        pv[i][1] = ld_dev(rs, pg + (q * 4 + sq) * 2048 + 1024);
      }
      char* orow = out + (size_t)(mtile * 128 + gt * 64 + (q & 1) * 32 + r32) * 2048 + 1024 + (ntile * 128 + ge * 64 + (q >> 1) * 32 + 8 * h) * 2;
#pragma unroll
      for (int r = 0; r < 2; ++r) {
        u32x4 o;
#pragma unroll
        for (int i = 0; i < 4; ++i) {
          float lo = own[8 * r + 2 * i], hi = own[8 * r + 2 * i + 1];
#pragma unroll
          for (int s = 0; s < 3; ++s) { lo += __uint_as_float(pv[s][r][i] << 16); hi += __uint_as_float(pv[s][r][i] & 0xffff0000u); }
          o[i] = pk(lo, hi);
        }
        *(u32x4*)(orow + 32 * r) = swap8(o);
      }
    }
    STAMPR(16); STAMPRT(17); STAMP_FLUSH(18); LST_FLUSH();
  }
}

extern "C" void kernel_launch(void* const* d_in, const int* in_sizes, int n_in, void* d_out, int out_size, void* d_ws, size_t ws_size,
                              hipStream_t stream) {
  (void)in_sizes; (void)n_in; (void)out_size; (void)ws_size;
  fused_mha_fnet<<<256, 768, 0, stream>>>((const char*)d_in[0], (const char*)d_in[1], (const char*)d_in[2], (const char*)d_in[3],
                                          (const char*)d_in[4], (const char*)d_in[5], (const char*)d_in[6], (const char*)d_in[7],
                                          (char*)d_ws, (char*)d_out);
}
```

```cpp
#include <hip/hip_runtime.h>
#include <stdint.h>

typedef __bf16 bf16x8  __attribute__((ext_vector_type(8)));
typedef __bf16 bf16x16 __attribute__((ext_vector_type(16)));
typedef __bf16 bf16x2  __attribute__((ext_vector_type(2)));
typedef short  s16x4   __attribute__((ext_vector_type(4)));
typedef short  s16x8   __attribute__((ext_vector_type(8)));
typedef float  f32x2   __attribute__((ext_vector_type(2)));
typedef float  f32x8   __attribute__((ext_vector_type(8)));
typedef float  f32x16  __attribute__((ext_vector_type(16)));
typedef unsigned u32x4 __attribute__((ext_vector_type(4)));
typedef unsigned u32x2 __attribute__((ext_vector_type(2)));
typedef unsigned long long u64;
#define LDSP __attribute__((address_space(3)))
#define GLBP __attribute__((address_space(1)))
#define DEV __device__ __forceinline__

constexpr uint32_t WS_Q  = 0;
constexpr uint32_t WS_KV = 2u << 20;
constexpr uint32_t WS_AB = (6u << 20) + 65536;
constexpr uint32_t WS_O  = WS_AB + (4u << 20);
constexpr uint32_t WS_P  = WS_O + (2u << 20);
constexpr uint32_t WS_F  = 24u << 20;
constexpr uint32_t WS_END = 25u << 20;
constexpr uint32_t FS = 8;
constexpr uint32_t F_E = WS_F, F_QKV = WS_F + 65536, F_AB = WS_F + 2 * 65536, F_O = WS_F + 3 * 65536, F_P = WS_F + 4 * 65536;
constexpr int SPIN_MAX = 1 << 18;

#define STAMP_DECL
#define STAMPR(i) do {} while (0)
#define STAMPRT(i) do {} while (0)
#define STAMP_FLUSH(n) do {} while (0)
#define SB __builtin_amdgcn_sched_barrier(0)
#define LST(i) do {} while (0)
#define LST_FLUSH() do {} while (0)
#define MFMA(a, b, c) __builtin_amdgcn_mfma_f32_32x32x16_bf16(a, b, c, 0, 0, 0)

DEV void glds(uint32_t ldsaddr, const void* sbase, uint32_t voff) {
  asm volatile("s_mov_b32 m0, %2\n\ts_nop 0\n\tglobal_load_lds_dwordx4 %0, %1" ::"v"(voff), "s"(sbase), "s"(ldsaddr) : "memory");
}
DEV void glds_dev(uint32_t ldsaddr, const void* sbase, uint32_t voff) {
  asm volatile("s_mov_b32 m0, %2\n\ts_nop 0\n\tglobal_load_lds_dwordx4 %0, %1 sc1" ::"v"(voff), "s"(sbase), "s"(ldsaddr) : "memory");
}
DEV unsigned warm(const char* a) {
  unsigned r;
  asm volatile("global_load_dword %0, %1, off" : "=&v"(r) : "v"(a) : "memory");
  return r;
}
#define KEEP(x) asm volatile("" ::"v"(x))
template <int N> DEV void wait_vm() { asm volatile("s_waitcnt vmcnt(%0)" ::"n"(N) : "memory"); }
DEV void barrier_lgkm() { asm volatile("s_waitcnt lgkmcnt(0)\n\ts_barrier" ::: "memory"); }
DEV bf16x8 ldsr(uint32_t a) { return *(const LDSP bf16x8*)(size_t)a; }
DEV bf16x8 ldstr(uint32_t a0, uint32_t a1) {
  s16x4 x = __builtin_amdgcn_ds_read_tr16_b64_v4i16((LDSP s16x4*)(size_t)a0);
  s16x4 y = __builtin_amdgcn_ds_read_tr16_b64_v4i16((LDSP s16x4*)(size_t)a1);
  return __builtin_bit_cast(bf16x8, (s16x8)__builtin_shufflevector(x, y, 0, 1, 2, 3, 4, 5, 6, 7));
}
DEV unsigned pk(float a, float b) {
  f32x2 v = {a, b};
  return __builtin_bit_cast(unsigned, __builtin_convertvector(v, bf16x2));
}
DEV u32x4 pk8(const f32x16& a, int o, float sc) {
  u32x4 r;
  r[0] = pk(a[o + 0] * sc, a[o + 1] * sc); r[1] = pk(a[o + 2] * sc, a[o + 3] * sc);
  r[2] = pk(a[o + 4] * sc, a[o + 5] * sc); r[3] = pk(a[o + 6] * sc, a[o + 7] * sc);
  return r;
}
DEV u32x4 pk8n(const f32x16& a, int o) {
  u32x4 r;
  r[0] = pk(a[o + 0], a[o + 1]); r[1] = pk(a[o + 2], a[o + 3]);
  r[2] = pk(a[o + 4], a[o + 5]); r[3] = pk(a[o + 6], a[o + 7]);
  return r;
}
DEV u32x4 swap8(u32x4 v) {
  auto a = __builtin_amdgcn_permlane32_swap(v[0], v[2], false, false);
  auto b = __builtin_amdgcn_permlane32_swap(v[1], v[3], false, false);
  u32x4 r; r[0] = a[0]; r[1] = b[0]; r[2] = a[1]; r[3] = b[1];
  return r;
}
DEV f32x16 splat16(float v) {
  f32x16 r;
#pragma unroll
  for (int i = 0; i < 16; ++i) r[i] = v;
  return r;
}

typedef __amdgpu_buffer_rsrc_t rsrc_t;
DEV void st_dev(rsrc_t r, uint32_t off, u32x4 v) { __builtin_amdgcn_raw_buffer_store_b128(v, r, off, 0, 16); }
DEV u32x4 ld_dev(rsrc_t r, uint32_t off) { return __builtin_amdgcn_raw_buffer_load_b128(r, off, 0, 16); }
DEV void st_out(char* p, u32x4 v);
DEV u64 uni64(u64 v) {
  const unsigned lo = __builtin_amdgcn_readfirstlane((unsigned)v), hi = __builtin_amdgcn_readfirstlane((unsigned)(v >> 32));
  return ((u64)hi << 32) | lo;
}
DEV void publish(uint32_t cnt, unsigned nwaves, char* ws, uint32_t flag, u64 tag, int l) {
  wait_vm<0>();
  unsigned old = 0;
  if (l == 0) old = __hip_atomic_fetch_add((LDSP unsigned*)(size_t)cnt, 1u, __ATOMIC_RELAXED, __HIP_MEMORY_SCOPE_WORKGROUP);
  old = __builtin_amdgcn_readfirstlane(old);
  if (old == nwaves - 1 && l == 0) __hip_atomic_store((GLBP u64*)(ws + flag), tag, __ATOMIC_RELAXED, __HIP_MEMORY_SCOPE_AGENT);
}
DEV void poll(const char* ws, uint32_t off, u64 tag) {
#pragma unroll 1
  for (int it = 0; it < SPIN_MAX; ++it) {
    const u64 v = __hip_atomic_load((const GLBP u64*)(ws + off), __ATOMIC_RELAXED, __HIP_MEMORY_SCOPE_AGENT);
    if (__builtin_amdgcn_ballot_w64(v != tag) == 0) break;
    __builtin_amdgcn_s_sleep(1);
  }
  asm volatile("" ::: "memory");
}

constexpr uint32_t KV0 = 0, SLOT = 2080, KVBUF = 4 * SLOT;
constexpr uint32_t G0 = 32768, GBUF = 32768;
constexpr uint32_t CNT = 28672;
#define SMF(a, b, c) __builtin_amdgcn_smfmac_f32_32x32x32_bf16(a, b, c, sidx, 0, 0)
#define LO8(x) __builtin_bit_cast(bf16x16, (f32x8)__builtin_shufflevector(x, x, 0, 1, 2, 3, 4, 5, 6, 7))
#define HI8(x) __builtin_bit_cast(bf16x16, (f32x8)__builtin_shufflevector(x, x, 8, 9, 10, 11, 12, 13, 14, 15))
#define PIN(x) do { asm volatile("" ::"v"(x)); SB; } while (0)
#define EXPR(s, a, b) do { _Pragma("unroll") for (int i_ = a; i_ < b; ++i_) s[i_] = __builtin_amdgcn_exp2f(s[i_]); SB; } while (0)

struct AttnState {
  f32x16 sA, sB, oA, oB, nA, nB;
  bf16x8 qA, qB, kf, v0, v1;
};

template <int BUFI, int U>
DEV void attn_step(AttnState& a, uint32_t aK, uint32_t aV0, uint32_t aV1, int sidx) {
  constexpr uint32_t OFF = BUFI * KVBUF + U * SLOT;
  EXPR(a.sA, 0, 2);
  a.oB = SMF(a.v1, HI8(a.sB), a.oB); PIN(a.oB);
  a.v1 = ldsr(aV1 + OFF); SB;
  EXPR(a.sA, 2, 7);
  a.sB = MFMA(a.kf, a.qB, a.nB); PIN(a.sB);
  a.kf = ldsr(aK + OFF); SB;
  EXPR(a.sA, 7, 12);
  a.oA = SMF(a.v0, LO8(a.sA), a.oA); PIN(a.oA);
  EXPR(a.sA, 12, 16);
  EXPR(a.sB, 0, 2);
  a.oA = SMF(a.v1, HI8(a.sA), a.oA); PIN(a.oA);
  EXPR(a.sB, 2, 7);
  a.sA = MFMA(a.kf, a.qA, a.nA); PIN(a.sA);
  EXPR(a.sB, 7, 12);
  a.oB = SMF(a.v0, LO8(a.sB), a.oB); PIN(a.oB);
  a.v0 = ldsr(aV0 + OFF); SB;
  EXPR(a.sB, 12, 16);
}
template <int BUFI>
DEV void attn_stage(AttnState& a, uint32_t aK, uint32_t aV0, uint32_t aV1, int sidx, int stg) {
  barrier_lgkm(); SB;
  LST(stg);
  attn_step<BUFI, 0>(a, aK, aV0, aV1, sidx);
  attn_step<BUFI, 1>(a, aK, aV0, aV1, sidx);
}

DEV float max16(const f32x16& s) {
  float m = fmaxf(fmaxf(fmaxf(s[0], s[1]), fmaxf(s[2], s[3])), fmaxf(fmaxf(s[4], s[5]), fmaxf(s[6], s[7])));
  float n = fmaxf(fmaxf(fmaxf(s[8], s[9]), fmaxf(s[10], s[11])), fmaxf(fmaxf(s[12], s[13]), fmaxf(s[14], s[15])));
  return fmaxf(m, n);
}
DEV float xhalf_max(float m) {
  auto t = __builtin_amdgcn_permlane32_swap(__float_as_uint(m), __float_as_uint(m), false, false);
  return fmaxf(__uint_as_float(t[0]), __uint_as_float(t[1]));
}
DEV bf16x8 ones8() { u32x4 o = {0x3F803F80u, 0x3F803F80u, 0x3F803F80u, 0x3F803F80u}; return __builtin_bit_cast(bf16x8, o); }
DEV bf16x8 ldf(rsrc_t r, uint32_t off) { return __builtin_bit_cast(bf16x8, ld_dev(r, off)); }

DEV f32x16 attn_exact(rsrc_t rs, uint32_t kvh, bf16x8 q, int l, int sidx) {
  const int h = l >> 5, r32 = l & 31;
  float m = -INFINITY;
  const f32x16 z = splat16(0.f);
#pragma unroll 1
  for (int t = 0; t < 64; ++t) {
    bf16x8 k = ldf(rs, kvh + t * 2048 + l * 16);
    f32x16 s = MFMA(k, q, z);
    m = fmaxf(m, max16(s));
  }
  m = xhalf_max(m);
  const f32x16 nm = splat16(-m);
  f32x16 o = z;
#pragma unroll 1
  for (int t = 0; t < 64; ++t) {
    bf16x8 k = ldf(rs, kvh + t * 2048 + l * 16);
    bf16x8 v0 = ldf(rs, kvh + t * 2048 + 1024 + h * 256 + (r32 & 15) * 16);
    bf16x8 v1 = ldf(rs, kvh + t * 2048 + 1536 + h * 256 + (r32 & 15) * 16);
    if (r32 >= 16) { v0 = ones8(); v1 = ones8(); }
    f32x16 s = MFMA(k, q, nm);
#pragma unroll
    for (int i = 0; i < 16; ++i) s[i] = __builtin_amdgcn_exp2f(s[i]);
    o = SMF(v0, LO8(s), o);
    o = SMF(v1, HI8(s), o);
  }
  return o;
}

#define XTLB 1
#define XOUT 0
#define DLY 2
#define GMODE 1
#define GPRE __builtin_amdgcn_s_setprio(0)
#define GPOST __builtin_amdgcn_s_setprio(GPRIO)
#define GGAP do {} while (0)
#define GPRIO 3
#define KVROT 0
#define CROT 0
#define AROT 0

DEV void st_out(char* p, u32x4 v) {
  *(u32x4*)p = v;
}
__global__ __launch_bounds__(768) void fused_mha_fnet(const char* __restrict__ xA, const char* __restrict__ wqkv, const char* __restrict__ wo,
                                                      const char* __restrict__ xB, const char* __restrict__ cre, const char* __restrict__ cim,
                                                      const char* __restrict__ hre, const char* __restrict__ him, char* __restrict__ ws,
                                                      char* __restrict__ out) {
  __shared__ __attribute__((aligned(1024))) char smem[163840];
  const uint32_t lds0 = (uint32_t)(size_t)(LDSP char*)smem;
  const int l = threadIdx.x & 63, w = __builtin_amdgcn_readfirstlane(threadIdx.x >> 6);
  const int h = l >> 5, r32 = l & 31;
  const int b = blockIdx.x, xcd = b & 7, idx = b >> 3;
  const int mt = (xcd >> 1) * 8 + (idx >> 2), nt = (xcd & 1) * 4 + (idx & 3);
  const int head = xcd * 4 + (idx >> 3), qblk = idx & 7;
  STAMP_DECL; STAMPRT(0); STAMPR(1);
  const rsrc_t rs = __builtin_amdgcn_make_buffer_rsrc(ws, 0, WS_END, 0x00020000);
  const uint32_t xrd = (uint32_t)r32 * 128u + (uint32_t)((h ^ ((r32 >> 1) & 7)) * 16);

  if (w < 8) {
    const u64 tag = uni64(__hip_atomic_load((const GLBP u64*)(ws + F_E), __ATOMIC_RELAXED, __HIP_MEMORY_SCOPE_AGENT)) + 1;
    const int wm = w & 1, wn = (w >> 1) & 1;
    unsigned tlbv = 0;
    if (w == 7 || (XTLB == 2 && w == 6)) {
      const int q_ = xcd >> 1, mtile_ = (xcd & 1) * 8 + (idx >> 2), ntile_ = idx & 3;
      const char* p = ws + F_QKV;
      if (l == 1) p = ws + WS_Q + (nt * 4 * 64 + mt * 2) * 1024;
      if (l == 2) p = ws + WS_KV + (nt * 4 * 64 + mt * 2) * 2048;
      if (l == 3) p = ws + WS_Q + (head * 64 + qblk * 8) * 1024;
      if (l == 4) p = ws + WS_KV + head * 131072;
      if (l == 5) p = ws + WS_AB + nt * (512u << 10) + (mt * 4) * 4096;
      if (l == 6) p = ws + WS_AB + (size_t)((q_ >> 1) * 4 + ntile_) * (512u << 10) + (size_t)(q_ & 1) * 64 * 4096;
      if (l == 7) p = ws + WS_O + ((qblk * 8) * 32 + head) * 1024;
      if (l == 8) p = ws + WS_O + (size_t)((xcd * 4 + (idx >> 3)) * 2) * 32768;
      if (l == 9) p = ws + WS_P + (((mtile_ * 4 + ntile_) * 4) * 16) * 2048;
      if (l == 10) p = out + (size_t)((xcd * 4 + (idx >> 3)) * 64) * 2048;
      if (l == 11) p = out + (size_t)(mtile_ * 128) * 2048;
      if (l == 12) p = (q_ < 2 ? cre : cim) + (size_t)(mtile_ * 128) * 4096;
      if (l == 13) p = wo;
      if (l == 14) p = (nt < 4 ? hre : him);
      if (l == 15) p = xB + (size_t)(mt * 64) * 1024;
      if (l < 16) asm volatile("global_load_dword %0, %1, off sc1" : "=&v"(tlbv) : "v"(p) : "memory");
    }
    f32x16 a0 = splat16(0.f), a1 = a0;
    {
      const int q = (l & 15) >> 2, pp = l & 3, blk = (l >> 4) & 1;
      const uint32_t rdX = lds0 + wm * 4096 + xrd;
      const uint32_t rdW = lds0 + 8192u + (w < 4 ? 0u : 16384u) + (uint32_t)(8 * h + q) * 128u +
                           (uint32_t)(((4 * wn + 2 * blk + (pp >> 1)) ^ (4 * (q >> 1))) * 16 + 8 * (pp & 1));
      uint32_t rslot = 0;
      if (w < 4) {
#pragma unroll 1
        for (int p = 0; p < 8; ++p) {
          barrier_lgkm();
          STAMPR(2);
          const uint32_t bx = rdX + rslot, bw = rdW + rslot;
          bf16x8 x[4], wq[4], wk[4];
#pragma unroll
          for (int ks = 0; ks < 4; ++ks) {
            x[ks] = ldsr(bx ^ (ks << 5));
            wq[ks] = ldstr(bw + ks * 2048, bw + ks * 2048 + 512);
            wk[ks] = ldstr(bw + 8192 + ks * 2048, bw + 8192 + ks * 2048 + 512);
          }
          SB;
#pragma unroll
          for (int ks = 0; ks < 4; ++ks) { a0 = MFMA(wq[ks], x[ks], a0); a1 = MFMA(wk[ks], x[ks], a1); }
          rslot = rslot == 65536 ? 0 : rslot + 32768;
        }
      } else {
#pragma unroll 1
        for (int p = 0; p < 8; ++p) {
          barrier_lgkm();
          STAMPR(2);
          const uint32_t bx = rdX + rslot, bw = rdW + rslot;
          bf16x8 x[4], wv[4];
#pragma unroll
          for (int ks = 0; ks < 4; ++ks) {
            x[ks] = ldsr(bx ^ (ks << 5));
            wv[ks] = ldstr(bw + ks * 2048, bw + ks * 2048 + 512);
          }
          SB;
#pragma unroll
          for (int ks = 0; ks < 4; ks += 2) { a0 = MFMA(x[ks], wv[ks], a0); a1 = MFMA(x[ks + 1], wv[ks + 1], a1); }
          rslot = rslot == 65536 ? 0 : rslot + 32768;
        }
#pragma unroll
        for (int i = 0; i < 16; ++i) a0[i] += a1[i];
      }
    }
    LST(31);
    if (w == 0) {
      if (l < 12) { u32x4 o = {0x3F803F80u, 0x3F803F80u, 0x3F803F80u, 0x3F803F80u}; *(LDSP u32x4*)(size_t)(lds0 + KV0 + l * SLOT + 2048) = o; }
      if (l < 16) *(LDSP unsigned*)(size_t)(lds0 + CNT + l * 4) = 0u;
    }
    {
      const uint32_t tile = mt * 2 + wm, head0 = nt * 4 + wn * 2;
      if (w < 4) {
        constexpr float QS = 0.25f * 1.4426950408889634f;
        const int pr = (r32 & 0x13) | ((r32 & 4) << 1) | ((r32 & 8) >> 1);
#pragma unroll
        for (int hh = 0; hh < 2; ++hh) {
          st_dev(rs, WS_Q + ((head0 + hh) * 64 + tile) * 1024 + l * 16, pk8(a0, 8 * hh, QS));
          st_dev(rs, WS_KV + ((head0 + hh) * 64 + tile) * 2048 + (h * 32 + pr) * 16, pk8n(a1, 8 * hh));
        }
      } else {
        const uint32_t vb = WS_KV + ((head0 + (r32 >> 4)) * 64 + tile) * 2048 + 1024 + h * 256 + (r32 & 15) * 16;
        st_dev(rs, vb, pk8n(a0, 0));
        st_dev(rs, vb + 512, pk8n(a0, 8));
      }
    }
    STAMPR(3); LST(33);
    barrier_lgkm();
    LST(34);
    wait_vm<0>();
    KEEP(tlbv);
    LST(35);
    publish(lds0 + CNT + 0, 8, ws, F_QKV + (nt * 32 + mt) * FS, tag, l);
    STAMPR(4); LST(36);
    barrier_lgkm();
    LST(37);
    if (w == 0) poll(ws, F_QKV + (xcd * 32 + r32) * FS, tag);
    STAMPR(5); LST(38);
    barrier_lgkm();
    STAMPR(6); LST(39);

    const int qg = w & 3, kh = w >> 2;
    const uint32_t kvh = WS_KV + head * 131072;
    int sidx = 0xDDDD;
    asm volatile("" : "+v"(sidx));
    AttnState a;
    {
      const uint32_t qf = WS_Q + (head * 64 + qblk * 8 + qg * 2) * 1024 + l * 16;
      a.qA = ldf(rs, qf);
      a.qB = ldf(rs, qf + 1024);
      const uint32_t kv1 = kvh + ((KVROT * qblk) & 15) * 4096;
      a.kf = ldf(rs, kv1 + kh * 65536 + l * 16);
      const bf16x8 k0 = ldf(rs, kv1 + l * 16);
      a.v0 = ldf(rs, kv1 + kh * 65536 + 1024 + h * 256 + (r32 & 15) * 16);
      if (r32 >= 16) a.v0 = ones8();
      const f32x16 z = splat16(0.f);
      f32x16 t0 = MFMA(k0, a.qA, z), t1 = MFMA(k0, a.qB, z);
      a.nA = splat16(-(xhalf_max(max16(t0)) + 64.f));
      a.nB = splat16(-(xhalf_max(max16(t1)) + 64.f));
      a.oA = z; a.oB = z; a.sB = z;
      a.v1 = __builtin_bit_cast(bf16x8, (u32x4){0u, 0u, 0u, 0u});
      a.sA = MFMA(a.kf, a.qA, a.nA);
    }
    const uint32_t sb = lds0 + KV0 + kh * 2 * SLOT;
    const bool onesl = (r32 >= 16 && r32 < 20) || r32 >= 28;
    const uint32_t vrow = h * 256 + (r32 < 16 ? r32 * 16 : 0);
    const uint32_t aK = sb + l * 16;
    const uint32_t aV0 = sb + (onesl ? 2048u : 1024u + vrow);
    const uint32_t aV1 = sb + (onesl ? 2048u : 1536u + vrow);
    STAMPR(7); LST(41);
#pragma unroll 1
    for (int it = 0; it < 5; ++it) {
      attn_stage<0>(a, aK, aV0, aV1, sidx, 3 * it);
      attn_stage<1>(a, aK, aV0, aV1, sidx, 3 * it + 1);
      attn_stage<2>(a, aK, aV0, aV1, sidx, 3 * it + 2);
    }
    attn_stage<0>(a, aK, aV0, aV1, sidx, 15);
    a.oB = SMF(a.v1, HI8(a.sB), a.oB);
    STAMPR(8);
    float lA = h ? a.oA[12] : a.oA[8], lB = h ? a.oB[12] : a.oB[8];
    const uint32_t cb = lds0 + KVBUF + qg * 4608 + l * 4;
    if (kh == 1) {
#pragma unroll
      for (int i = 0; i < 8; ++i) {
        *(LDSP float*)(size_t)(cb + i * 256) = a.oA[i];
        *(LDSP float*)(size_t)(cb + (9 + i) * 256) = a.oB[i];
      }
      *(LDSP float*)(size_t)(cb + 8 * 256) = lA;
      *(LDSP float*)(size_t)(cb + 17 * 256) = lB;
    }
    LST(15 + 1);
    barrier_lgkm();
    LST(17);
    const int st = xcd * 4 + (idx >> 3), n3 = idx & 7;
    if (kh == 0) {
      float oa[8], ob[8];
#pragma unroll
      for (int i = 0; i < 8; ++i) {
        oa[i] = a.oA[i] + *(const LDSP float*)(size_t)(cb + i * 256);
        ob[i] = a.oB[i] + *(const LDSP float*)(size_t)(cb + (9 + i) * 256);
      }
      lA += *(const LDSP float*)(size_t)(cb + 8 * 256);
      lB += *(const LDSP float*)(size_t)(cb + 17 * 256);
      bool okA = lA >= 0x1p-100f && lA <= 0x1p120f, okB = lB >= 0x1p-100f && lB <= 0x1p120f;
#pragma unroll
      for (int i = 0; i < 8; ++i) { okA = okA && (fabsf(oa[i]) <= 0x1p126f); okB = okB && (fabsf(ob[i]) <= 0x1p126f); }
      if (__builtin_amdgcn_ballot_w64(!okA) != 0) {
        f32x16 o = attn_exact(rs, kvh, a.qA, l, sidx);
#pragma unroll
        for (int i = 0; i < 8; ++i) oa[i] = o[i];
        lA = h ? o[12] : o[8];
      }
      if (__builtin_amdgcn_ballot_w64(!okB) != 0) {
        f32x16 o = attn_exact(rs, kvh, a.qB, l, sidx);
#pragma unroll
        for (int i = 0; i < 8; ++i) ob[i] = o[i];
        lB = h ? o[12] : o[8];
      }
      LST(19);
      const float rA = 1.0f / lA, rB = 1.0f / lB;
      u32x4 pa, pb;
#pragma unroll
      for (int i = 0; i < 4; ++i) { pa[i] = pk(oa[2 * i] * rA, oa[2 * i + 1] * rA); pb[i] = pk(ob[2 * i] * rB, ob[2 * i + 1] * rB); }
      const uint32_t of = WS_O + ((qblk * 8 + qg * 2) * 32 + head) * 1024 + l * 16;
      st_dev(rs, of, pa);
      st_dev(rs, of + 32768, pb);
      STAMPR(9); LST(20);
      barrier_lgkm();
      LST(21);
      barrier_lgkm();
      LST(22);
      wait_vm<0>();
      LST(23);
      publish(lds0 + CNT + 8, 4, ws, F_O + (qblk * 32 + head) * FS, tag, l);
      STAMPR(10); LST(24);
      barrier_lgkm();
      LST(25);
      barrier_lgkm();
      STAMPR(11); LST(26);
      const int w0 = w & 1, w1 = w >> 1;
      const int q4 = (l & 15) >> 2, p4 = l & 3, blk = (l >> 4) & 1;
      const uint32_t rdW = lds0 + G0 + 16384u + (uint32_t)(4 * h + q4) * 128u +
                           (uint32_t)(((4 * w1 + 2 * blk + (p4 >> 1)) ^ (4 * (q4 >> 1))) * 16 + 8 * (p4 & 1));
      const uint32_t rdO = lds0 + G0 + w0 * 8192 + l * 16;
      f32x16 c0 = splat16(0.f), c1 = c0;
#pragma unroll 1
      for (int j = 0; j < 4; ++j) {
#pragma unroll 1
        for (int it = 0; it < SPIN_MAX; ++it) {
          const unsigned v = __hip_atomic_load((LDSP unsigned*)(size_t)(lds0 + CNT + 16 + j * 4), __ATOMIC_RELAXED, __HIP_MEMORY_SCOPE_WORKGROUP);
          if (__builtin_amdgcn_readfirstlane(v) != 0) break;
          __builtin_amdgcn_s_sleep(1);
        }
        asm volatile("" ::: "memory");
        LST(27 + j);
#pragma unroll
        for (int i = 0; i < 8; i += 2) {
          bf16x8 o0 = ldsr(rdO + j * GBUF + i * 1024), o1 = ldsr(rdO + j * GBUF + i * 1024 + 1024);
          bf16x8 f0 = ldstr(rdW + j * GBUF + i * 2048, rdW + j * GBUF + i * 2048 + 1024);
          bf16x8 f1 = ldstr(rdW + j * GBUF + i * 2048 + 2048, rdW + j * GBUF + i * 2048 + 3072);
          c0 = MFMA(f0, o0, c0);
          c1 = MFMA(f1, o1, c1);
        }
      }
      STAMPR(12);
#pragma unroll
      for (int i = 0; i < 16; ++i) c0[i] += c1[i];
      char* orow = out + (size_t)(st * 64 + w0 * 32 + r32) * 2048 + (n3 * 64 + w1 * 32 + 8 * h) * 2;
      st_out(orow, swap8(pk8n(c0, 0)));
      st_out(orow + 32, swap8(pk8n(c0, 8)));
      if (b == 0 && threadIdx.x == 0) __hip_atomic_store((GLBP u64*)(ws + F_E), tag, __ATOMIC_RELAXED, __HIP_MEMORY_SCOPE_AGENT);
    } else {
      const int j = w - 4;
      const uint32_t voW = (uint32_t)(l >> 3) * 1024u + (uint32_t)(((l & 7) ^ (((l >> 4) & 1) << 2)) * 16);
      const char* wg = wo + (size_t)(128 * j) * 1024 + n3 * 128;
      const uint32_t d = lds0 + G0 + j * GBUF;
      auto issueW = [&]() {
#pragma unroll
        for (int i = 0; i < 16; ++i) glds(d + 16384 + i * 1024, wg + (size_t)(8 * i) * 1024, voW);
      };
      barrier_lgkm();
      LST(21);
      if (j == 0) issueW();
      LST(18);
      barrier_lgkm();
      LST(22);
      if (j == 1) issueW();
      barrier_lgkm();
      LST(25);
      if (j == 2) issueW();
      barrier_lgkm();
      LST(26);
      if (j == 3) issueW();
      STAMPR(9); LST(27);
      poll(ws, F_O + ((st >> 2) * 32 + 8 * j + (l & 7)) * FS, tag);
      STAMPR(10); LST(28);
      const char* og = ws + WS_O + (size_t)(st * 2) * 32768 + (8 * j) * 1024;
#pragma unroll
      for (int i = 0; i < 16; ++i) glds_dev(d + i * 1024, og + (size_t)(i >> 3) * 32768 + (i & 7) * 1024, l * 16);
      LST(29);
      wait_vm<0>();
      STAMPR(11); LST(30);
      if (l == 0) __hip_atomic_store((LDSP unsigned*)(size_t)(lds0 + CNT + 16 + j * 4), 1u, __ATOMIC_RELAXED, __HIP_MEMORY_SCOPE_WORKGROUP);
      STAMPR(12);
    }
    STAMPR(13); STAMPRT(14); STAMP_FLUSH(15); LST_FLUSH();
  } else {
    const int g = w - 8;
    __builtin_amdgcn_s_setprio(GPRIO);
    unsigned pfa, pfb, pfc, pfd;
    u64 eraw;
    asm volatile("global_load_dwordx2 %0, %1, %2 sc1" : "=&v"(eraw) : "v"(0u), "s"(ws + F_E) : "memory");
    {
      const int lx = l & 15, lw = l < 40 ? (l + 8) % 24 : 0;
      const char* px = xA + (size_t)(mt * 64 + (nt & 3) * 16 + lx) * 1024;
      const char* pw = wqkv + (size_t)((mt & 7) * 8 + (lw & 7)) * 3072 + (lw >> 3) * 1024 + nt * 128;
      const bool isx = l < 16;
      pfa = warm(isx ? px + g * 128 : pw + (size_t)g * (64 * 3072));
      pfb = warm(isx ? px + (g + 4) * 128 : pw + (size_t)(g + 4) * (64 * 3072));
    }
    const char* hw = nt < 4 ? hre : him;
    const uint32_t vo0 = (uint32_t)(l >> 3) * 1024u + (uint32_t)(((l & 7) ^ (l >> 4)) * 16);
    const uint32_t vo1 = (uint32_t)(8 + (l >> 3)) * 1024u + (uint32_t)(((l & 7) ^ (4 + (l >> 4))) * 16);
    {
      const char* gx = xA + (size_t)(mt * 64 + 16 * g) * 1024;
      const char* gw = wqkv + (size_t)(16 * g) * 3072 + nt * 128;
      const uint32_t vw = (uint32_t)(l >> 3) * 3072u + (uint32_t)(((l & 7) ^ (((l >> 4) & 1) << 2)) * 16);
      auto issue = [&](int p, uint32_t slot) {
        const uint32_t bb = lds0 + slot + g * 2048;
        glds(bb, gx + p * 128, vo0);
        glds(bb + 1024, gx + p * 128, vo1);
#pragma unroll
        for (int c = 0; c < 3; ++c) {
          glds(bb + 8192 + c * 8192, gw + (size_t)p * (64 * 3072) + c * 1024, vw);
          glds(bb + 8192 + c * 8192 + 1024, gw + (size_t)p * (64 * 3072) + c * 1024 + 8 * 3072, vw);
        }
      };
      for (int i = 0; i < DLY; ++i) __builtin_amdgcn_s_sleep(4);
      issue(0, 0); issue(1, 32768);
      wait_vm<16>();
      asm volatile("" : "+v"(eraw));
      KEEP(pfa); KEEP(pfb);
      uint32_t wslot = 65536;
#pragma unroll 1
      for (int p = 0; p < 6; ++p) {
        wait_vm<8>();
        asm volatile("s_barrier" ::: "memory");
        issue(p + 2, wslot);
        wslot = wslot == 65536 ? 0 : wslot + 32768;
      }
    }
    const u64 tag = uni64(eraw) + 1;
    {
      const char* px = xB + (size_t)(mt * 64 + (nt & 3) * 16 + (l & 15)) * 1024;
      const char* pw = hw + (size_t)((nt & 3) * 128 + (mt & 7) * 16 + (l & 15)) * 1024;
      pfc = warm((l < 32 ? px : pw) + g * 256 + ((l >> 4) & 1) * 128);
    }
    wait_vm<9>();
    barrier_lgkm();
    wait_vm<1>();
    barrier_lgkm();
    STAMPR(2);
    const char* hx = xB + (size_t)(mt * 64 + 16 * g) * 1024;
    const char* hwg = hw + (size_t)((nt & 3) * 128 + 32 * g) * 1024;
    constexpr uint32_t HB = 114688, HA = 65536, HS = 32768, HSUB = 24576;
    auto issueH = [&](int hs, uint32_t slot) {
      const uint32_t d = lds0 + slot;
      glds(d + (16 * g) * 128, hx + hs * 128, vo0);
      glds(d + (16 * g + 8) * 128, hx + hs * 128, vo1);
      glds(d + 8192 + (32 * g) * 128, hwg + hs * 128, vo0);
      glds(d + 8192 + (32 * g + 8) * 128, hwg + hs * 128, vo1);
      glds(d + 8192 + (32 * g + 16) * 128, hwg + 16384 + hs * 128, vo0);
      glds(d + 8192 + (32 * g + 24) * 128, hwg + 16384 + hs * 128, vo1);
    };
    f32x16 c00 = splat16(0.f), c01 = c00, c10 = c00, c11 = c00;
    const uint32_t hrX = lds0 + xrd, hrE = lds0 + 8192 + g * 4096 + xrd;
    auto computeH = [&](uint32_t slot) {
#pragma unroll
      for (int ks = 0; ks < 4; ++ks) {
        bf16x8 x0 = ldsr((hrX + slot) ^ (ks << 5)), x1 = ldsr(((hrX + slot) ^ (ks << 5)) + 4096);
        bf16x8 e = ldsr((hrE + slot) ^ (ks << 5));
        c00 = MFMA(x0, e, c00);
        c01 = MFMA(x1, e, c01);
      }
    };
    const char* kvb = ws + WS_KV + (size_t)head * 131072 + (g >> 1) * 65536;
    auto issueKV = [&](int p, uint32_t buf) {
      const uint32_t d = lds0 + KV0 + buf + g * SLOT;
      const uint32_t t = 2 * ((p + KVROT * qblk) & 15) + (g & 1), nx = (t + 1) & 31;
      glds_dev(d, kvb + nx * 2048, l * 16);
      glds_dev(d + 1024, kvb, 1024 + l * 16 + (l < 32 ? nx : t) * 2048);
    };
    barrier_lgkm();
    issueH(0, HB); issueH(1, HB + HSUB); issueH(2, HA); issueH(3, HA + HSUB);
    wait_vm<12>();
    KEEP(pfc);
    barrier_lgkm();
    STAMPR(3);
    computeH(HB); computeH(HB + HSUB);
    wait_vm<0>();
    STAMPR(4);
    barrier_lgkm();
    STAMPR(5);
    issueKV(0, 0); issueKV(1, KVBUF);
    issueH(4, HB); issueH(5, HB + HSUB); issueH(6, HS);
    computeH(HA); computeH(HA + HSUB);
    wait_vm<20>();
    STAMPR(6);
    barrier_lgkm();
    issueH(7, HA); issueKV(2, 2 * KVBUF);
    wait_vm<2>();
    barrier_lgkm();
    STAMPR(7);
    issueKV(3, 0);
    computeH(HB); computeH(HB + HSUB); computeH(HS); computeH(HA);
    {
      const float sg = nt < 4 ? 1.f : -1.f;
      const uint32_t ab = WS_AB + nt * (512u << 10) + (mt * 4) * 4096 + g * 1024 + l * 16;
      st_dev(rs, ab, swap8(pk8(c00, 0, sg)));
      st_dev(rs, ab + 4096, swap8(pk8(c00, 8, sg)));
      st_dev(rs, ab + 8192, swap8(pk8(c01, 0, sg)));
      st_dev(rs, ab + 12288, swap8(pk8(c01, 8, sg)));
    }
    wait_vm<6>();
    STAMPR(8);
    barrier_lgkm();
    publish(lds0 + CNT + 4, 4, ws, F_AB + (nt * 32 + mt) * FS, tag, l);
    STAMPR(9);
    const int ge = g & 1, gt = g >> 1;
    const int q = xcd >> 1, mtile = (xcd & 1) * 8 + (idx >> 2), ntile = idx & 3;
    const char* gbase; uint32_t pstride, qstride, go0, go1;
    if (g < 2) {
      gbase = (q < 2 ? cre : cim) + (size_t)(mtile * 128 + g * 64) * 4096 + (q & 1) * 2048;
      pstride = 128; qstride = 65536;
      go0 = (uint32_t)(l >> 3) * 4096u + (uint32_t)(((l & 7) ^ (l >> 4)) * 16);
      go1 = (uint32_t)(8 + (l >> 3)) * 4096u + (uint32_t)(((l & 7) ^ (4 + (l >> 4))) * 16);
    } else {
      gbase = ws + WS_AB + (size_t)((q >> 1) * 4 + ntile) * (512u << 10) + (size_t)(q & 1) * 64 * 4096 + (g - 2) * 8192;
      pstride = 16384; qstride = 2048;
      go0 = l * 16; go1 = l * 16 + 1024;
    }
    const int krot = CROT * ntile + AROT * (mtile & 7);
    auto issueG2 = [&](int p, int i0) {
      const char* sb = gbase + (size_t)((p + krot) & 15) * pstride + (size_t)(i0 >> 1) * qstride;
      const uint32_t d = lds0 + G0 + (p & 3) * GBUF + g * 8192 + i0 * 1024;
      glds_dev(d, sb, go0);
      glds_dev(d + 1024, sb, go1);
    };
    auto issueG = [&](int p) { issueG2(p, 0); issueG2(p, 2); issueG2(p, 4); issueG2(p, 6); };
    pfd = warm(wo + (size_t)((idx >> 3) * 128 + g * 32 + r32) * 1024 + (idx & 7) * 128);
    issueKV(4, KVBUF);
    if (g < 2) { issueG(0); issueG(1); issueG(2); }
    barrier_lgkm();
    issueKV(5, 2 * KVBUF);
    if (g >= 2) {
      poll(ws, F_AB + (((q >> 1) * 4 + ntile) * 32 + (q & 1) * 16 + (l & 15)) * FS, tag);
      issueG(0); issueG(1); issueG(2);
      wait_vm<16>();
    } else {
      wait_vm<18>();
    }
    KEEP(pfd);
    STAMPR(10);
    const uint32_t rdC = lds0 + G0 + (uint32_t)(64 * gt) * 128u + xrd;
    const uint32_t rdA = lds0 + G0 + 16384 + ge * 2048 + l * 16;
    c00 = splat16(0.f); c01 = c00; c10 = c00; c11 = c00;
    uint32_t kvbuf = 0;
#pragma unroll 1
    for (int p = 4; p < 20; ++p) {
      barrier_lgkm();
      LST(4 * (p - 4));
      if (p == 4) STAMPR(11);
      if (p == 16) STAMPR(12);
      const uint32_t so = (p & 3) * GBUF;
      const uint32_t ra = rdA + so, rc = rdC + so;
      bf16x8 e0 = ldsr(ra), e1 = ldsr(ra + 1024), t0 = ldsr(rc), t1 = ldsr(rc + 4096);
      if (p <= 13) issueKV(p + 2, kvbuf);
      kvbuf = kvbuf == 2 * KVBUF ? 0 : kvbuf + KVBUF;
      LST(4 * (p - 4) + 1);
#pragma unroll
      for (int ks = 0; ks < 4; ++ks) {
        bf16x8 f0, f1, u0, u1;
        if (ks < 3) {
          f0 = ldsr(ra + (ks + 1) * 4096); f1 = ldsr(ra + (ks + 1) * 4096 + 1024);
          u0 = ldsr(rc ^ ((ks + 1) << 5)); u1 = ldsr((rc ^ ((ks + 1) << 5)) + 4096);
        }
        SB;
        GPRE;
        c00 = MFMA(e0, t0, c00); GGAP; c01 = MFMA(e0, t1, c01); GGAP;
        c10 = MFMA(e1, t0, c10); GGAP; c11 = MFMA(e1, t1, c11); GGAP;
        GPOST;
        SB;
        if (p <= 16) issueG2(p - 1, 2 * ks);
        if (ks < 3) { e0 = f0; e1 = f1; t0 = u0; t1 = u1; }
      }
      LST(4 * (p - 4) + 2);
      if (p == 4) { if (g < 2) wait_vm<10>(); else wait_vm<18>(); }
      else if (p <= 13) wait_vm<18>();
      else if (p <= 16) wait_vm<16>();
      else if (p == 17) wait_vm<8>();
      else wait_vm<0>();
      LST(4 * (p - 4) + 3);
    }
    barrier_lgkm();
    STAMPR(13);
    const uint32_t pg = WS_P + (((mtile * 4 + ntile) * 4 + g) * 16) * 2048 + l * 16;
    f32x16 own;
    auto send = [&](const f32x16& c, int dq) {
      st_dev(rs, pg + (dq * 4 + q) * 2048, pk8n(c, 0));
      st_dev(rs, pg + (dq * 4 + q) * 2048 + 1024, pk8n(c, 8));
    };
    if (q != 0) send(c00, 0);
    if (q != 1) send(c01, 1);
    if (q != 2) send(c10, 2);
    if (q != 3) send(c11, 3);
    if (q == 0) own = c00; else if (q == 1) own = c01; else if (q == 2) own = c10; else own = c11;
    publish(lds0 + CNT + 12, 4, ws, F_P + ((mtile * 4 + ntile) * 4 + q) * FS, tag, l);
    STAMPR(14);
    poll(ws, F_P + ((mtile * 4 + ntile) * 4 + (l & 3)) * FS, tag);
    STAMPR(15);
    {
      u32x4 pv[3][2];
#pragma unroll
      for (int i = 0; i < 3; ++i) {
        const int sq = (q + 1 + i) & 3;
        pv[i][0] = ld_dev(rs, pg + (q * 4 + sq) * 2048);
        pv[i][1] = ld_dev(rs, pg + (q * 4 + sq) * 2048 + 1024);
      }
      char* orow = out + (size_t)(mtile * 128 + gt * 64 + (q & 1) * 32 + r32) * 2048 + 1024 + (ntile * 128 + ge * 64 + (q >> 1) * 32 + 8 * h) * 2;
#pragma unroll
      for (int r = 0; r < 2; ++r) {
        u32x4 o;
#pragma unroll
        for (int i = 0; i < 4; ++i) {
          float lo = own[8 * r + 2 * i], hi = own[8 * r + 2 * i + 1];
#pragma unroll
          for (int s = 0; s < 3; ++s) { lo += __uint_as_float(pv[s][r][i] << 16); hi += __uint_as_float(pv[s][r][i] & 0xffff0000u); }
          o[i] = pk(lo, hi);
        }
        st_out(orow + 32 * r, swap8(o));
      }
    }
    STAMPR(16); STAMPRT(17); STAMP_FLUSH(18); LST_FLUSH();
  }
}

extern "C" void kernel_launch(void* const* d_in, const int* in_sizes, int n_in, void* d_out, int out_size, void* d_ws, size_t ws_size,
                              hipStream_t stream) {
  (void)in_sizes; (void)n_in; (void)out_size; (void)ws_size;
  fused_mha_fnet<<<256, 768, 0, stream>>>((const char*)d_in[0], (const char*)d_in[1], (const char*)d_in[2], (const char*)d_in[3],
                                          (const char*)d_in[4], (const char*)d_in[5], (const char*)d_in[6], (const char*)d_in[7],
                                          (char*)d_ws, (char*)d_out);
}
```

```cpp
#include <hip/hip_runtime.h>
#include <stdint.h>

typedef __bf16 bf16x8  __attribute__((ext_vector_type(8)));
typedef __bf16 bf16x16 __attribute__((ext_vector_type(16)));
typedef __bf16 bf16x2  __attribute__((ext_vector_type(2)));
typedef short  s16x4   __attribute__((ext_vector_type(4)));
typedef short  s16x8   __attribute__((ext_vector_type(8)));
typedef float  f32x2   __attribute__((ext_vector_type(2)));
typedef float  f32x8   __attribute__((ext_vector_type(8)));
typedef float  f32x16  __attribute__((ext_vector_type(16)));
typedef unsigned u32x4 __attribute__((ext_vector_type(4)));
typedef unsigned u32x2 __attribute__((ext_vector_type(2)));
typedef unsigned long long u64;
#define LDSP __attribute__((address_space(3)))
#define GLBP __attribute__((address_space(1)))
#define DEV __device__ __forceinline__

constexpr uint32_t WS_Q  = 0;
constexpr uint32_t WS_KV = 2u << 20;
constexpr uint32_t WS_AB = (6u << 20) + 65536;
constexpr uint32_t WS_O  = WS_AB + (4u << 20);
constexpr uint32_t WS_P  = WS_O + (2u << 20);
constexpr uint32_t WS_F  = 24u << 20;
constexpr uint32_t WS_END = 25u << 20;
constexpr uint32_t FS = 8;
constexpr uint32_t F_E = WS_F, F_QKV = WS_F + 65536, F_AB = WS_F + 2 * 65536, F_O = WS_F + 3 * 65536, F_P = WS_F + 4 * 65536;
constexpr int SPIN_MAX = 1 << 18;

#define STAMP_DECL
#define STAMPR(i) do {} while (0)
#define STAMPRT(i) do {} while (0)
#define STAMP_FLUSH(n) do {} while (0)
#define SB __builtin_amdgcn_sched_barrier(0)
#define LST(i) do {} while (0)
#define LST_FLUSH() do {} while (0)
#define MFMA(a, b, c) __builtin_amdgcn_mfma_f32_32x32x16_bf16(a, b, c, 0, 0, 0)

DEV void glds(uint32_t ldsaddr, const void* sbase, uint32_t voff) {
  asm volatile("s_mov_b32 m0, %2\n\ts_nop 0\n\tglobal_load_lds_dwordx4 %0, %1" ::"v"(voff), "s"(sbase), "s"(ldsaddr) : "memory");
}
DEV void glds_dev(uint32_t ldsaddr, const void* sbase, uint32_t voff) {
  asm volatile("s_mov_b32 m0, %2\n\ts_nop 0\n\tglobal_load_lds_dwordx4 %0, %1 sc1" ::"v"(voff), "s"(sbase), "s"(ldsaddr) : "memory");
}
DEV unsigned warm(const char* a) {
  unsigned r;
  asm volatile("global_load_dword %0, %1, off" : "=&v"(r) : "v"(a) : "memory");
  return r;
}
#define KEEP(x) asm volatile("" ::"v"(x))
template <int N> DEV void wait_vm() { asm volatile("s_waitcnt vmcnt(%0)" ::"n"(N) : "memory"); }
DEV void barrier_lgkm() { asm volatile("s_waitcnt lgkmcnt(0)\n\ts_barrier" ::: "memory"); }
DEV bf16x8 ldsr(uint32_t a) { return *(const LDSP bf16x8*)(size_t)a; }
DEV bf16x8 ldstr(uint32_t a0, uint32_t a1) {
  s16x4 x = __builtin_amdgcn_ds_read_tr16_b64_v4i16((LDSP s16x4*)(size_t)a0);
  s16x4 y = __builtin_amdgcn_ds_read_tr16_b64_v4i16((LDSP s16x4*)(size_t)a1);
  return __builtin_bit_cast(bf16x8, (s16x8)__builtin_shufflevector(x, y, 0, 1, 2, 3, 4, 5, 6, 7));
}
DEV unsigned pk(float a, float b) {
  f32x2 v = {a, b};
  return __builtin_bit_cast(unsigned, __builtin_convertvector(v, bf16x2));
}
DEV u32x4 pk8(const f32x16& a, int o, float sc) {
  u32x4 r;
  r[0] = pk(a[o + 0] * sc, a[o + 1] * sc); r[1] = pk(a[o + 2] * sc, a[o + 3] * sc);
  r[2] = pk(a[o + 4] * sc, a[o + 5] * sc); r[3] = pk(a[o + 6] * sc, a[o + 7] * sc);
  return r;
}
DEV u32x4 pk8n(const f32x16& a, int o) {
  u32x4 r;
  r[0] = pk(a[o + 0], a[o + 1]); r[1] = pk(a[o + 2], a[o + 3]);
  r[2] = pk(a[o + 4], a[o + 5]); r[3] = pk(a[o + 6], a[o + 7]);
  return r;
}
DEV u32x4 swap8(u32x4 v) {
  auto a = __builtin_amdgcn_permlane32_swap(v[0], v[2], false, false);
  auto b = __builtin_amdgcn_permlane32_swap(v[1], v[3], false, false);
  u32x4 r; r[0] = a[0]; r[1] = b[0]; r[2] = a[1]; r[3] = b[1];
  return r;
}
DEV f32x16 splat16(float v) {
  f32x16 r;
#pragma unroll
  for (int i = 0; i < 16; ++i) r[i] = v;
  return r;
}

typedef __amdgpu_buffer_rsrc_t rsrc_t;
DEV void st_dev(rsrc_t r, uint32_t off, u32x4 v) { __builtin_amdgcn_raw_buffer_store_b128(v, r, off, 0, 16); }
DEV u32x4 ld_dev(rsrc_t r, uint32_t off) { return __builtin_amdgcn_raw_buffer_load_b128(r, off, 0, 16); }
DEV u64 uni64(u64 v) {
  const unsigned lo = __builtin_amdgcn_readfirstlane((unsigned)v), hi = __builtin_amdgcn_readfirstlane((unsigned)(v >> 32));
  return ((u64)hi << 32) | lo;
}
DEV void publish(uint32_t cnt, unsigned nwaves, char* ws, uint32_t flag, u64 tag, int l) {
  wait_vm<0>();
  unsigned old = 0;
  if (l == 0) old = __hip_atomic_fetch_add((LDSP unsigned*)(size_t)cnt, 1u, __ATOMIC_RELAXED, __HIP_MEMORY_SCOPE_WORKGROUP);
  old = __builtin_amdgcn_readfirstlane(old);
  if (old == nwaves - 1 && l == 0) __hip_atomic_store((GLBP u64*)(ws + flag), tag, __ATOMIC_RELAXED, __HIP_MEMORY_SCOPE_AGENT);
}
DEV void poll(const char* ws, uint32_t off, u64 tag) {
#pragma unroll 1
  for (int it = 0; it < SPIN_MAX; ++it) {
    const u64 v = __hip_atomic_load((const GLBP u64*)(ws + off), __ATOMIC_RELAXED, __HIP_MEMORY_SCOPE_AGENT);
    if (__builtin_amdgcn_ballot_w64(v != tag) == 0) break;
    __builtin_amdgcn_s_sleep(1);
  }
  asm volatile("" ::: "memory");
}

constexpr uint32_t KV0 = 0, SLOT = 2080, KVBUF = 4 * SLOT;
constexpr uint32_t G0 = 32768, GBUF = 32768;
constexpr uint32_t CNT = 28672;
#define SMF(a, b, c) __builtin_amdgcn_smfmac_f32_32x32x32_bf16(a, b, c, sidx, 0, 0)
#define LO8(x) __builtin_bit_cast(bf16x16, (f32x8)__builtin_shufflevector(x, x, 0, 1, 2, 3, 4, 5, 6, 7))
#define HI8(x) __builtin_bit_cast(bf16x16, (f32x8)__builtin_shufflevector(x, x, 8, 9, 10, 11, 12, 13, 14, 15))
#define PIN(x) do { asm volatile("" ::"v"(x)); SB; } while (0)
#define EXPR(s, a, b) do { _Pragma("unroll") for (int i_ = a; i_ < b; ++i_) s[i_] = __builtin_amdgcn_exp2f(s[i_]); SB; } while (0)

struct AttnState {
  f32x16 sA, sB, oA, oB, nA, nB;
  bf16x8 qA, qB, kf, v0, v1;
};

template <int BUFI, int U>
DEV void attn_step(AttnState& a, uint32_t aK, uint32_t aV0, uint32_t aV1, int sidx) {
  constexpr uint32_t OFF = BUFI * KVBUF + U * SLOT;
  EXPR(a.sA, 0, 2);
  a.oB = SMF(a.v1, HI8(a.sB), a.oB); PIN(a.oB);
  a.v1 = ldsr(aV1 + OFF); SB;
  EXPR(a.sA, 2, 7);
  a.sB = MFMA(a.kf, a.qB, a.nB); PIN(a.sB);
  a.kf = ldsr(aK + OFF); SB;
  EXPR(a.sA, 7, 12);
  a.oA = SMF(a.v0, LO8(a.sA), a.oA); PIN(a.oA);
  EXPR(a.sA, 12, 16);
  EXPR(a.sB, 0, 2);
  a.oA = SMF(a.v1, HI8(a.sA), a.oA); PIN(a.oA);
  EXPR(a.sB, 2, 7);
  a.sA = MFMA(a.kf, a.qA, a.nA); PIN(a.sA);
  EXPR(a.sB, 7, 12);
  a.oB = SMF(a.v0, LO8(a.sB), a.oB); PIN(a.oB);
  a.v0 = ldsr(aV0 + OFF); SB;
  EXPR(a.sB, 12, 16);
}
template <int BUFI>
DEV void attn_stage(AttnState& a, uint32_t aK, uint32_t aV0, uint32_t aV1, int sidx, int stg) {
  barrier_lgkm(); SB;
  LST(stg);
  attn_step<BUFI, 0>(a, aK, aV0, aV1, sidx);
  attn_step<BUFI, 1>(a, aK, aV0, aV1, sidx);
}

DEV float max16(const f32x16& s) {
  float m = fmaxf(fmaxf(fmaxf(s[0], s[1]), fmaxf(s[2], s[3])), fmaxf(fmaxf(s[4], s[5]), fmaxf(s[6], s[7])));
  float n = fmaxf(fmaxf(fmaxf(s[8], s[9]), fmaxf(s[10], s[11])), fmaxf(fmaxf(s[12], s[13]), fmaxf(s[14], s[15])));
  return fmaxf(m, n);
}
DEV float xhalf_max(float m) {
  auto t = __builtin_amdgcn_permlane32_swap(__float_as_uint(m), __float_as_uint(m), false, false);
  return fmaxf(__uint_as_float(t[0]), __uint_as_float(t[1]));
}
DEV bf16x8 ones8() { u32x4 o = {0x3F803F80u, 0x3F803F80u, 0x3F803F80u, 0x3F803F80u}; return __builtin_bit_cast(bf16x8, o); }
DEV bf16x8 ldf(rsrc_t r, uint32_t off) { return __builtin_bit_cast(bf16x8, ld_dev(r, off)); }

DEV f32x16 attn_exact(rsrc_t rs, uint32_t kvh, bf16x8 q, int l, int sidx) {
  const int h = l >> 5, r32 = l & 31;
  float m = -INFINITY;
  const f32x16 z = splat16(0.f);
#pragma unroll 1
  for (int t = 0; t < 64; ++t) {
    bf16x8 k = ldf(rs, kvh + t * 2048 + l * 16);
    f32x16 s = MFMA(k, q, z);
    m = fmaxf(m, max16(s));
  }
  m = xhalf_max(m);
  const f32x16 nm = splat16(-m);
  f32x16 o = z;
#pragma unroll 1
  for (int t = 0; t < 64; ++t) {
    bf16x8 k = ldf(rs, kvh + t * 2048 + l * 16);
    bf16x8 v0 = ldf(rs, kvh + t * 2048 + 1024 + h * 256 + (r32 & 15) * 16);
    bf16x8 v1 = ldf(rs, kvh + t * 2048 + 1536 + h * 256 + (r32 & 15) * 16);
    if (r32 >= 16) { v0 = ones8(); v1 = ones8(); }
    f32x16 s = MFMA(k, q, nm);
#pragma unroll
    for (int i = 0; i < 16; ++i) s[i] = __builtin_amdgcn_exp2f(s[i]);
    o = SMF(v0, LO8(s), o);
    o = SMF(v1, HI8(s), o);
  }
  return o;
}

#define XTLB 1
#define XOUT 0
#define DLY 2
#define GMODE 1
#define GPRE __builtin_amdgcn_s_setprio(0)
#define GPOST __builtin_amdgcn_s_setprio(GPRIO)
#define GGAP do {} while (0)
#define GPRIO 3
#define KVROT 0
#define CROT 0
#define AROT 0

#define OAUX (XOUT == 0 ? 0 : XOUT == 1 ? 17 : XOUT == 2 ? 2 : XOUT == 3 ? 16 : 18)
DEV void st_out(rsrc_t r, uint32_t off, u32x4 v) { __builtin_amdgcn_raw_buffer_store_b128(v, r, off, 0, OAUX); }
__global__ __launch_bounds__(768) void fused_mha_fnet(const char* __restrict__ xA, const char* __restrict__ wqkv, const char* __restrict__ wo,
                                                      const char* __restrict__ xB, const char* __restrict__ cre, const char* __restrict__ cim,
                                                      const char* __restrict__ hre, const char* __restrict__ him, char* __restrict__ ws,
                                                      char* __restrict__ out) {
  __shared__ __attribute__((aligned(1024))) char smem[163840];
  const uint32_t lds0 = (uint32_t)(size_t)(LDSP char*)smem;
  const int l = threadIdx.x & 63, w = __builtin_amdgcn_readfirstlane(threadIdx.x >> 6);
  const int h = l >> 5, r32 = l & 31;
  const int b = blockIdx.x, xcd = b & 7, idx = b >> 3;
  const int mt = (xcd >> 1) * 8 + (idx >> 2), nt = (xcd & 1) * 4 + (idx & 3);
  const int head = xcd * 4 + (idx >> 3), qblk = idx & 7;
  STAMP_DECL; STAMPRT(0); STAMPR(1);
  const rsrc_t rs = __builtin_amdgcn_make_buffer_rsrc(ws, 0, WS_END, 0x00020000);
  const uint32_t xrd = (uint32_t)r32 * 128u + (uint32_t)((h ^ ((r32 >> 1) & 7)) * 16);

  if (w < 8) {
    const u64 tag = uni64(__hip_atomic_load((const GLBP u64*)(ws + F_E), __ATOMIC_RELAXED, __HIP_MEMORY_SCOPE_AGENT)) + 1;
    const int wm = w & 1, wn = (w >> 1) & 1;
    unsigned tlbv = 0;
    if (w == 7 || (XTLB == 2 && w == 6)) {
      const int q_ = xcd >> 1, mtile_ = (xcd & 1) * 8 + (idx >> 2), ntile_ = idx & 3;
      const char* p = ws + F_QKV;
      if (l == 1) p = ws + WS_Q + (nt * 4 * 64 + mt * 2) * 1024;
      if (l == 2) p = ws + WS_KV + (nt * 4 * 64 + mt * 2) * 2048;
      if (l == 3) p = ws + WS_Q + (head * 64 + qblk * 8) * 1024;
      if (l == 4) p = ws + WS_KV + head * 131072;
      if (l == 5) p = ws + WS_AB + nt * (512u << 10) + (mt * 4) * 4096;
      if (l == 6) p = ws + WS_AB + (size_t)((q_ >> 1) * 4 + ntile_) * (512u << 10) + (size_t)(q_ & 1) * 64 * 4096;
      if (l == 7) p = ws + WS_O + ((qblk * 8) * 32 + head) * 1024;
      if (l == 8) p = ws + WS_O + (size_t)((xcd * 4 + (idx >> 3)) * 2) * 32768;
      if (l == 9) p = ws + WS_P + (((mtile_ * 4 + ntile_) * 4) * 16) * 2048;
      if (l == 10) p = out + (size_t)((xcd * 4 + (idx >> 3)) * 64) * 2048;
      if (l == 11) p = out + (size_t)(mtile_ * 128) * 2048;
      if (l == 12) p = (q_ < 2 ? cre : cim) + (size_t)(mtile_ * 128) * 4096;
      if (l == 13) p = wo;
      if (l == 14) p = (nt < 4 ? hre : him);
      if (l == 15) p = xB + (size_t)(mt * 64) * 1024;
      if (l < 16) asm volatile("global_load_dword %0, %1, off sc1" : "=&v"(tlbv) : "v"(p) : "memory");
    }
    f32x16 a0 = splat16(0.f), a1 = a0;
    {
      const int q = (l & 15) >> 2, pp = l & 3, blk = (l >> 4) & 1;
      const uint32_t rdX = lds0 + wm * 4096 + xrd;
      const uint32_t rdW = lds0 + 8192u + (w < 4 ? 0u : 16384u) + (uint32_t)(8 * h + q) * 128u +
                           (uint32_t)(((4 * wn + 2 * blk + (pp >> 1)) ^ (4 * (q >> 1))) * 16 + 8 * (pp & 1));
      uint32_t rslot = 0;
      if (w < 4) {
#pragma unroll 1
        for (int p = 0; p < 8; ++p) {
          barrier_lgkm();
          STAMPR(2);
          const uint32_t bx = rdX + rslot, bw = rdW + rslot;
          bf16x8 x[4], wq[4], wk[4];
#pragma unroll
          for (int ks = 0; ks < 4; ++ks) {
            x[ks] = ldsr(bx ^ (ks << 5));
            wq[ks] = ldstr(bw + ks * 2048, bw + ks * 2048 + 512);
            wk[ks] = ldstr(bw + 8192 + ks * 2048, bw + 8192 + ks * 2048 + 512);
          }
          SB;
#pragma unroll
          for (int ks = 0; ks < 4; ++ks) { a0 = MFMA(wq[ks], x[ks], a0); a1 = MFMA(wk[ks], x[ks], a1); }
          rslot = rslot == 65536 ? 0 : rslot + 32768;
        }
      } else {
#pragma unroll 1
        for (int p = 0; p < 8; ++p) {
          barrier_lgkm();
          STAMPR(2);
          const uint32_t bx = rdX + rslot, bw = rdW + rslot;
          bf16x8 x[4], wv[4];
#pragma unroll
          for (int ks = 0; ks < 4; ++ks) {
            x[ks] = ldsr(bx ^ (ks << 5));
            wv[ks] = ldstr(bw + ks * 2048, bw + ks * 2048 + 512);
          }
          SB;
#pragma unroll
          for (int ks = 0; ks < 4; ks += 2) { a0 = MFMA(x[ks], wv[ks], a0); a1 = MFMA(x[ks + 1], wv[ks + 1], a1); }
          rslot = rslot == 65536 ? 0 : rslot + 32768;
        }
#pragma unroll
        for (int i = 0; i < 16; ++i) a0[i] += a1[i];
      }
    }
    LST(31);
    if (w == 0) {
      if (l < 12) { u32x4 o = {0x3F803F80u, 0x3F803F80u, 0x3F803F80u, 0x3F803F80u}; *(LDSP u32x4*)(size_t)(lds0 + KV0 + l * SLOT + 2048) = o; }
      if (l < 16) *(LDSP unsigned*)(size_t)(lds0 + CNT + l * 4) = 0u;
    }
    {
      const uint32_t tile = mt * 2 + wm, head0 = nt * 4 + wn * 2;
      if (w < 4) {
        constexpr float QS = 0.25f * 1.4426950408889634f;
        const int pr = (r32 & 0x13) | ((r32 & 4) << 1) | ((r32 & 8) >> 1);
#pragma unroll
        for (int hh = 0; hh < 2; ++hh) {
          st_dev(rs, WS_Q + ((head0 + hh) * 64 + tile) * 1024 + l * 16, pk8(a0, 8 * hh, QS));
          st_dev(rs, WS_KV + ((head0 + hh) * 64 + tile) * 2048 + (h * 32 + pr) * 16, pk8n(a1, 8 * hh));
        }
      } else {
        const uint32_t vb = WS_KV + ((head0 + (r32 >> 4)) * 64 + tile) * 2048 + 1024 + h * 256 + (r32 & 15) * 16;
        st_dev(rs, vb, pk8n(a0, 0));
        st_dev(rs, vb + 512, pk8n(a0, 8));
      }
    }
    STAMPR(3); LST(33);
    barrier_lgkm();
    LST(34);
    wait_vm<0>();
    KEEP(tlbv);
    LST(35);
    publish(lds0 + CNT + 0, 8, ws, F_QKV + (nt * 32 + mt) * FS, tag, l);
    STAMPR(4); LST(36);
    barrier_lgkm();
    LST(37);
    if (w == 0) poll(ws, F_QKV + (xcd * 32 + r32) * FS, tag);
    STAMPR(5); LST(38);
    barrier_lgkm();
    STAMPR(6); LST(39);

    const int qg = w & 3, kh = w >> 2;
    const uint32_t kvh = WS_KV + head * 131072;
    int sidx = 0xDDDD;
    asm volatile("" : "+v"(sidx));
    AttnState a;
    {
      const uint32_t qf = WS_Q + (head * 64 + qblk * 8 + qg * 2) * 1024 + l * 16;
      a.qA = ldf(rs, qf);
      a.qB = ldf(rs, qf + 1024);
      const uint32_t kv1 = kvh + ((KVROT * qblk) & 15) * 4096;
      a.kf = ldf(rs, kv1 + kh * 65536 + l * 16);
      const bf16x8 k0 = ldf(rs, kv1 + l * 16);
      a.v0 = ldf(rs, kv1 + kh * 65536 + 1024 + h * 256 + (r32 & 15) * 16);
      if (r32 >= 16) a.v0 = ones8();
      const f32x16 z = splat16(0.f);
      f32x16 t0 = MFMA(k0, a.qA, z), t1 = MFMA(k0, a.qB, z);
      a.nA = splat16(-(xhalf_max(max16(t0)) + 64.f));
      a.nB = splat16(-(xhalf_max(max16(t1)) + 64.f));
      a.oA = z; a.oB = z; a.sB = z;
      a.v1 = __builtin_bit_cast(bf16x8, (u32x4){0u, 0u, 0u, 0u});
      a.sA = MFMA(a.kf, a.qA, a.nA);
    }
    const uint32_t sb = lds0 + KV0 + kh * 2 * SLOT;
    const bool onesl = (r32 >= 16 && r32 < 20) || r32 >= 28;
    const uint32_t vrow = h * 256 + (r32 < 16 ? r32 * 16 : 0);
    const uint32_t aK = sb + l * 16;
    const uint32_t aV0 = sb + (onesl ? 2048u : 1024u + vrow);
    const uint32_t aV1 = sb + (onesl ? 2048u : 1536u + vrow);
    STAMPR(7); LST(41);
#pragma unroll 1
    for (int it = 0; it < 5; ++it) {
      attn_stage<0>(a, aK, aV0, aV1, sidx, 3 * it);
      attn_stage<1>(a, aK, aV0, aV1, sidx, 3 * it + 1);
      attn_stage<2>(a, aK, aV0, aV1, sidx, 3 * it + 2);
    }
    attn_stage<0>(a, aK, aV0, aV1, sidx, 15);
    a.oB = SMF(a.v1, HI8(a.sB), a.oB);
    STAMPR(8);
    float lA = h ? a.oA[12] : a.oA[8], lB = h ? a.oB[12] : a.oB[8];
    const uint32_t cb = lds0 + KVBUF + qg * 4608 + l * 4;
    if (kh == 1) {
#pragma unroll
      for (int i = 0; i < 8; ++i) {
        *(LDSP float*)(size_t)(cb + i * 256) = a.oA[i];
        *(LDSP float*)(size_t)(cb + (9 + i) * 256) = a.oB[i];
      }
      *(LDSP float*)(size_t)(cb + 8 * 256) = lA;
      *(LDSP float*)(size_t)(cb + 17 * 256) = lB;
    }
    LST(15 + 1);
    barrier_lgkm();
    LST(17);
    const int st = xcd * 4 + (idx >> 3), n3 = idx & 7;
    if (kh == 0) {
      float oa[8], ob[8];
#pragma unroll
      for (int i = 0; i < 8; ++i) {
        oa[i] = a.oA[i] + *(const LDSP float*)(size_t)(cb + i * 256);
        ob[i] = a.oB[i] + *(const LDSP float*)(size_t)(cb + (9 + i) * 256);
      }
      lA += *(const LDSP float*)(size_t)(cb + 8 * 256);
      lB += *(const LDSP float*)(size_t)(cb + 17 * 256);
      bool okA = lA >= 0x1p-100f && lA <= 0x1p120f, okB = lB >= 0x1p-100f && lB <= 0x1p120f;
#pragma unroll
      for (int i = 0; i < 8; ++i) { okA = okA && (fabsf(oa[i]) <= 0x1p126f); okB = okB && (fabsf(ob[i]) <= 0x1p126f); }
      if (__builtin_amdgcn_ballot_w64(!okA) != 0) {
        f32x16 o = attn_exact(rs, kvh, a.qA, l, sidx);
#pragma unroll
        for (int i = 0; i < 8; ++i) oa[i] = o[i];
        lA = h ? o[12] : o[8];
      }
      if (__builtin_amdgcn_ballot_w64(!okB) != 0) {
        f32x16 o = attn_exact(rs, kvh, a.qB, l, sidx);
#pragma unroll
        for (int i = 0; i < 8; ++i) ob[i] = o[i];
        lB = h ? o[12] : o[8];
      }
      LST(19);
      const float rA = 1.0f / lA, rB = 1.0f / lB;
      u32x4 pa, pb;
#pragma unroll
      for (int i = 0; i < 4; ++i) { pa[i] = pk(oa[2 * i] * rA, oa[2 * i + 1] * rA); pb[i] = pk(ob[2 * i] * rB, ob[2 * i + 1] * rB); }
      const uint32_t of = WS_O + ((qblk * 8 + qg * 2) * 32 + head) * 1024 + l * 16;
      st_dev(rs, of, pa);
      st_dev(rs, of + 32768, pb);
      STAMPR(9); LST(20);
      barrier_lgkm();
      LST(21);
      barrier_lgkm();
      LST(22);
      wait_vm<0>();
      LST(23);
      publish(lds0 + CNT + 8, 4, ws, F_O + (qblk * 32 + head) * FS, tag, l);
      STAMPR(10); LST(24);
      barrier_lgkm();
      LST(25);
      barrier_lgkm();
      STAMPR(11); LST(26);
      const int w0 = w & 1, w1 = w >> 1;
      const int q4 = (l & 15) >> 2, p4 = l & 3, blk = (l >> 4) & 1;
      const uint32_t rdW = lds0 + G0 + 16384u + (uint32_t)(4 * h + q4) * 128u +
                           (uint32_t)(((4 * w1 + 2 * blk + (p4 >> 1)) ^ (4 * (q4 >> 1))) * 16 + 8 * (p4 & 1));
      const uint32_t rdO = lds0 + G0 + w0 * 8192 + l * 16;
      f32x16 c0 = splat16(0.f), c1 = c0;
#pragma unroll 1
      for (int j = 0; j < 4; ++j) {
#pragma unroll 1
        for (int it = 0; it < SPIN_MAX; ++it) {
          const unsigned v = __hip_atomic_load((LDSP unsigned*)(size_t)(lds0 + CNT + 16 + j * 4), __ATOMIC_RELAXED, __HIP_MEMORY_SCOPE_WORKGROUP);
          if (__builtin_amdgcn_readfirstlane(v) != 0) break;
          __builtin_amdgcn_s_sleep(1);
        }
        asm volatile("" ::: "memory");
        LST(27 + j);
#pragma unroll
        for (int i = 0; i < 8; i += 2) {
          bf16x8 o0 = ldsr(rdO + j * GBUF + i * 1024), o1 = ldsr(rdO + j * GBUF + i * 1024 + 1024);
          bf16x8 f0 = ldstr(rdW + j * GBUF + i * 2048, rdW + j * GBUF + i * 2048 + 1024);
          bf16x8 f1 = ldstr(rdW + j * GBUF + i * 2048 + 2048, rdW + j * GBUF + i * 2048 + 3072);
          c0 = MFMA(f0, o0, c0);
          c1 = MFMA(f1, o1, c1);
        }
      }
      STAMPR(12);
#pragma unroll
      for (int i = 0; i < 16; ++i) c0[i] += c1[i];
      const rsrc_t ro = __builtin_amdgcn_make_buffer_rsrc(out, 0, 4u << 20, 0x00020000);
      const uint32_t orow = (uint32_t)(st * 64 + w0 * 32 + r32) * 2048 + (n3 * 64 + w1 * 32 + 8 * h) * 2;
      st_out(ro, orow, swap8(pk8n(c0, 0)));
      st_out(ro, orow + 32, swap8(pk8n(c0, 8)));
      if (b == 0 && threadIdx.x == 0) __hip_atomic_store((GLBP u64*)(ws + F_E), tag, __ATOMIC_RELAXED, __HIP_MEMORY_SCOPE_AGENT);
    } else {
      const int j = w - 4;
      const uint32_t voW = (uint32_t)(l >> 3) * 1024u + (uint32_t)(((l & 7) ^ (((l >> 4) & 1) << 2)) * 16);
      const char* wg = wo + (size_t)(128 * j) * 1024 + n3 * 128;
      const uint32_t d = lds0 + G0 + j * GBUF;
      auto issueW = [&]() {
#pragma unroll
        for (int i = 0; i < 16; ++i) glds(d + 16384 + i * 1024, wg + (size_t)(8 * i) * 1024, voW);
      };
      barrier_lgkm();
      LST(21);
      if (j == 0) issueW();
      LST(18);
      barrier_lgkm();
      LST(22);
      if (j == 1) issueW();
      barrier_lgkm();
      LST(25);
      if (j == 2) issueW();
      barrier_lgkm();
      LST(26);
      if (j == 3) issueW();
      STAMPR(9); LST(27);
      poll(ws, F_O + ((st >> 2) * 32 + 8 * j + (l & 7)) * FS, tag);
      STAMPR(10); LST(28);
      const char* og = ws + WS_O + (size_t)(st * 2) * 32768 + (8 * j) * 1024;
#pragma unroll
      for (int i = 0; i < 16; ++i) glds_dev(d + i * 1024, og + (size_t)(i >> 3) * 32768 + (i & 7) * 1024, l * 16);
      LST(29);
      wait_vm<0>();
      STAMPR(11); LST(30);
      if (l == 0) __hip_atomic_store((LDSP unsigned*)(size_t)(lds0 + CNT + 16 + j * 4), 1u, __ATOMIC_RELAXED, __HIP_MEMORY_SCOPE_WORKGROUP);
      STAMPR(12);
    }
    STAMPR(13); STAMPRT(14); STAMP_FLUSH(15); LST_FLUSH();
  } else {
    const int g = w - 8;
    __builtin_amdgcn_s_setprio(GPRIO);
    unsigned pfa, pfb, pfc, pfd;
    u64 eraw;
    asm volatile("global_load_dwordx2 %0, %1, %2 sc1" : "=&v"(eraw) : "v"(0u), "s"(ws + F_E) : "memory");
    {
      const int lx = l & 15, lw = l < 40 ? (l + 8) % 24 : 0;
      const char* px = xA + (size_t)(mt * 64 + (nt & 3) * 16 + lx) * 1024;
      const char* pw = wqkv + (size_t)((mt & 7) * 8 + (lw & 7)) * 3072 + (lw >> 3) * 1024 + nt * 128;
      const bool isx = l < 16;
      pfa = warm(isx ? px + g * 128 : pw + (size_t)g * (64 * 3072));
      pfb = warm(isx ? px + (g + 4) * 128 : pw + (size_t)(g + 4) * (64 * 3072));
    }
    const char* hw = nt < 4 ? hre : him;
    const uint32_t vo0 = (uint32_t)(l >> 3) * 1024u + (uint32_t)(((l & 7) ^ (l >> 4)) * 16);
    const uint32_t vo1 = (uint32_t)(8 + (l >> 3)) * 1024u + (uint32_t)(((l & 7) ^ (4 + (l >> 4))) * 16);
    {
      const char* gx = xA + (size_t)(mt * 64 + 16 * g) * 1024;
      const char* gw = wqkv + (size_t)(16 * g) * 3072 + nt * 128;
      const uint32_t vw = (uint32_t)(l >> 3) * 3072u + (uint32_t)(((l & 7) ^ (((l >> 4) & 1) << 2)) * 16);
      auto issue = [&](int p, uint32_t slot) {
        const uint32_t bb = lds0 + slot + g * 2048;
        glds(bb, gx + p * 128, vo0);
        glds(bb + 1024, gx + p * 128, vo1);
#pragma unroll
        for (int c = 0; c < 3; ++c) {
          glds(bb + 8192 + c * 8192, gw + (size_t)p * (64 * 3072) + c * 1024, vw);
          glds(bb + 8192 + c * 8192 + 1024, gw + (size_t)p * (64 * 3072) + c * 1024 + 8 * 3072, vw);
        }
      };
      for (int i = 0; i < DLY; ++i) __builtin_amdgcn_s_sleep(4);
      issue(0, 0); issue(1, 32768);
      wait_vm<16>();
      asm volatile("" : "+v"(eraw));
      KEEP(pfa); KEEP(pfb);
      uint32_t wslot = 65536;
#pragma unroll 1
      for (int p = 0; p < 6; ++p) {
        wait_vm<8>();
        asm volatile("s_barrier" ::: "memory");
        issue(p + 2, wslot);
        wslot = wslot == 65536 ? 0 : wslot + 32768;
      }
    }
    const u64 tag = uni64(eraw) + 1;
    {
      const char* px = xB + (size_t)(mt * 64 + (nt & 3) * 16 + (l & 15)) * 1024;
      const char* pw = hw + (size_t)((nt & 3) * 128 + (mt & 7) * 16 + (l & 15)) * 1024;
      pfc = warm((l < 32 ? px : pw) + g * 256 + ((l >> 4) & 1) * 128);
    }
    wait_vm<9>();
    barrier_lgkm();
    wait_vm<1>();
    barrier_lgkm();
    STAMPR(2);
    const char* hx = xB + (size_t)(mt * 64 + 16 * g) * 1024;
    const char* hwg = hw + (size_t)((nt & 3) * 128 + 32 * g) * 1024;
    constexpr uint32_t HB = 114688, HA = 65536, HS = 32768, HSUB = 24576;
    auto issueH = [&](int hs, uint32_t slot) {
      const uint32_t d = lds0 + slot;
      glds(d + (16 * g) * 128, hx + hs * 128, vo0);
      glds(d + (16 * g + 8) * 128, hx + hs * 128, vo1);
      glds(d + 8192 + (32 * g) * 128, hwg + hs * 128, vo0);
      glds(d + 8192 + (32 * g + 8) * 128, hwg + hs * 128, vo1);
      glds(d + 8192 + (32 * g + 16) * 128, hwg + 16384 + hs * 128, vo0);
      glds(d + 8192 + (32 * g + 24) * 128, hwg + 16384 + hs * 128, vo1);
    };
    f32x16 c00 = splat16(0.f), c01 = c00, c10 = c00, c11 = c00;
    const uint32_t hrX = lds0 + xrd, hrE = lds0 + 8192 + g * 4096 + xrd;
    auto computeH = [&](uint32_t slot) {
#pragma unroll
      for (int ks = 0; ks < 4; ++ks) {
        bf16x8 x0 = ldsr((hrX + slot) ^ (ks << 5)), x1 = ldsr(((hrX + slot) ^ (ks << 5)) + 4096);
        bf16x8 e = ldsr((hrE + slot) ^ (ks << 5));
        c00 = MFMA(x0, e, c00);
        c01 = MFMA(x1, e, c01);
      }
    };
    const char* kvb = ws + WS_KV + (size_t)head * 131072 + (g >> 1) * 65536;
    auto issueKV = [&](int p, uint32_t buf) {
      const uint32_t d = lds0 + KV0 + buf + g * SLOT;
      const uint32_t t = 2 * ((p + KVROT * qblk) & 15) + (g & 1), nx = (t + 1) & 31;
      glds_dev(d, kvb + nx * 2048, l * 16);
      glds_dev(d + 1024, kvb, 1024 + l * 16 + (l < 32 ? nx : t) * 2048);
    };
    barrier_lgkm();
    issueH(0, HB); issueH(1, HB + HSUB); issueH(2, HA); issueH(3, HA + HSUB);
    wait_vm<12>();
    KEEP(pfc);
    barrier_lgkm();
    STAMPR(3);
    computeH(HB); computeH(HB + HSUB);
    wait_vm<0>();
    STAMPR(4);
    barrier_lgkm();
    STAMPR(5);
    issueKV(0, 0); issueKV(1, KVBUF);
    issueH(4, HB); issueH(5, HB + HSUB); issueH(6, HS);
    computeH(HA); computeH(HA + HSUB);
    wait_vm<20>();
    STAMPR(6);
    barrier_lgkm();
    issueH(7, HA); issueKV(2, 2 * KVBUF);
    wait_vm<2>();
    barrier_lgkm();
    STAMPR(7);
    issueKV(3, 0);
    computeH(HB); computeH(HB + HSUB); computeH(HS); computeH(HA);
    {
      const float sg = nt < 4 ? 1.f : -1.f;
      const uint32_t ab = WS_AB + nt * (512u << 10) + (mt * 4) * 4096 + g * 1024 + l * 16;
      st_dev(rs, ab, swap8(pk8(c00, 0, sg)));
      st_dev(rs, ab + 4096, swap8(pk8(c00, 8, sg)));
      st_dev(rs, ab + 8192, swap8(pk8(c01, 0, sg)));
      st_dev(rs, ab + 12288, swap8(pk8(c01, 8, sg)));
    }
    wait_vm<6>();
    STAMPR(8);
    barrier_lgkm();
    publish(lds0 + CNT + 4, 4, ws, F_AB + (nt * 32 + mt) * FS, tag, l);
    STAMPR(9);
    const int ge = g & 1, gt = g >> 1;
    const int q = xcd >> 1, mtile = (xcd & 1) * 8 + (idx >> 2), ntile = idx & 3;
    const char* gbase; uint32_t pstride, qstride, go0, go1;
    if (g < 2) {
      gbase = (q < 2 ? cre : cim) + (size_t)(mtile * 128 + g * 64) * 4096 + (q & 1) * 2048;
      pstride = 128; qstride = 65536;
      go0 = (uint32_t)(l >> 3) * 4096u + (uint32_t)(((l & 7) ^ (l >> 4)) * 16);
      go1 = (uint32_t)(8 + (l >> 3)) * 4096u + (uint32_t)(((l & 7) ^ (4 + (l >> 4))) * 16);
    } else {
      gbase = ws + WS_AB + (size_t)((q >> 1) * 4 + ntile) * (512u << 10) + (size_t)(q & 1) * 64 * 4096 + (g - 2) * 8192;
      pstride = 16384; qstride = 2048;
      go0 = l * 16; go1 = l * 16 + 1024;
    }
    const int krot = CROT * ntile + AROT * (mtile & 7);
    auto issueG2 = [&](int p, int i0) {
      const char* sb = gbase + (size_t)((p + krot) & 15) * pstride + (size_t)(i0 >> 1) * qstride;
      const uint32_t d = lds0 + G0 + (p & 3) * GBUF + g * 8192 + i0 * 1024;
      glds_dev(d, sb, go0);
      glds_dev(d + 1024, sb, go1);
    };
    auto issueG = [&](int p) { issueG2(p, 0); issueG2(p, 2); issueG2(p, 4); issueG2(p, 6); };
    pfd = warm(wo + (size_t)((idx >> 3) * 128 + g * 32 + r32) * 1024 + (idx & 7) * 128);
    issueKV(4, KVBUF);
    if (g < 2) { issueG(0); issueG(1); issueG(2); }
    barrier_lgkm();
    issueKV(5, 2 * KVBUF);
    if (g >= 2) {
      poll(ws, F_AB + (((q >> 1) * 4 + ntile) * 32 + (q & 1) * 16 + (l & 15)) * FS, tag);
      issueG(0); issueG(1); issueG(2);
      wait_vm<16>();
    } else {
      wait_vm<18>();
    }
    KEEP(pfd);
    STAMPR(10);
    const uint32_t rdC = lds0 + G0 + (uint32_t)(64 * gt) * 128u + xrd;
    const uint32_t rdA = lds0 + G0 + 16384 + ge * 2048 + l * 16;
    c00 = splat16(0.f); c01 = c00; c10 = c00; c11 = c00;
    uint32_t kvbuf = 0;
#pragma unroll 1
    for (int p = 4; p < 20; ++p) {
      barrier_lgkm();
      LST(4 * (p - 4));
      if (p == 4) STAMPR(11);
      if (p == 16) STAMPR(12);
      const uint32_t so = (p & 3) * GBUF;
      const uint32_t ra = rdA + so, rc = rdC + so;
      bf16x8 e0 = ldsr(ra), e1 = ldsr(ra + 1024), t0 = ldsr(rc), t1 = ldsr(rc + 4096);
      if (p <= 13) issueKV(p + 2, kvbuf);
      kvbuf = kvbuf == 2 * KVBUF ? 0 : kvbuf + KVBUF;
      LST(4 * (p - 4) + 1);
#pragma unroll
      for (int ks = 0; ks < 4; ++ks) {
        bf16x8 f0, f1, u0, u1;
        if (ks < 3) {
          f0 = ldsr(ra + (ks + 1) * 4096); f1 = ldsr(ra + (ks + 1) * 4096 + 1024);
          u0 = ldsr(rc ^ ((ks + 1) << 5)); u1 = ldsr((rc ^ ((ks + 1) << 5)) + 4096);
        }
        SB;
        GPRE;
        c00 = MFMA(e0, t0, c00); GGAP; c01 = MFMA(e0, t1, c01); GGAP;
        c10 = MFMA(e1, t0, c10); GGAP; c11 = MFMA(e1, t1, c11); GGAP;
        GPOST;
        SB;
        if (p <= 16) issueG2(p - 1, 2 * ks);
        if (ks < 3) { e0 = f0; e1 = f1; t0 = u0; t1 = u1; }
      }
      LST(4 * (p - 4) + 2);
      if (p == 4) { if (g < 2) wait_vm<10>(); else wait_vm<18>(); }
      else if (p <= 13) wait_vm<18>();
      else if (p <= 16) wait_vm<16>();
      else if (p == 17) wait_vm<8>();
      else wait_vm<0>();
      LST(4 * (p - 4) + 3);
    }
    barrier_lgkm();
    STAMPR(13);
    const uint32_t pg = WS_P + (((mtile * 4 + ntile) * 4 + g) * 16) * 2048 + l * 16;
    f32x16 own;
    auto send = [&](const f32x16& c, int dq) {
      st_dev(rs, pg + (dq * 4 + q) * 2048, pk8n(c, 0));
      st_dev(rs, pg + (dq * 4 + q) * 2048 + 1024, pk8n(c, 8));
    };
    if (q != 0) send(c00, 0);
    if (q != 1) send(c01, 1);
    if (q != 2) send(c10, 2);
    if (q != 3) send(c11, 3);
    if (q == 0) own = c00; else if (q == 1) own = c01; else if (q == 2) own = c10; else own = c11;
    publish(lds0 + CNT + 12, 4, ws, F_P + ((mtile * 4 + ntile) * 4 + q) * FS, tag, l);
    STAMPR(14);
    poll(ws, F_P + ((mtile * 4 + ntile) * 4 + (l & 3)) * FS, tag);
    STAMPR(15);
    {
      u32x4 pv[3][2];
#pragma unroll
      for (int i = 0; i < 3; ++i) {
        const int sq = (q + 1 + i) & 3;
        pv[i][0] = ld_dev(rs, pg + (q * 4 + sq) * 2048);
        pv[i][1] = ld_dev(rs, pg + (q * 4 + sq) * 2048 + 1024);
      }
      const rsrc_t ro = __builtin_amdgcn_make_buffer_rsrc(out, 0, 4u << 20, 0x00020000);
      const uint32_t orow = (uint32_t)(mtile * 128 + gt * 64 + (q & 1) * 32 + r32) * 2048 + 1024 + (ntile * 128 + ge * 64 + (q >> 1) * 32 + 8 * h) * 2;
#pragma unroll
      for (int r = 0; r < 2; ++r) {
        u32x4 o;
#pragma unroll
        for (int i = 0; i < 4; ++i) {
          float lo = own[8 * r + 2 * i], hi = own[8 * r + 2 * i + 1];
#pragma unroll
          for (int s = 0; s < 3; ++s) { lo += __uint_as_float(pv[s][r][i] << 16); hi += __uint_as_float(pv[s][r][i] & 0xffff0000u); }
          o[i] = pk(lo, hi);
        }
        st_out(ro, orow + 32 * r, swap8(o));
      }
    }
    STAMPR(16); STAMPRT(17); STAMP_FLUSH(18); LST_FLUSH();
  }
}

extern "C" void kernel_launch(void* const* d_in, const int* in_sizes, int n_in, void* d_out, int out_size, void* d_ws, size_t ws_size,
                              hipStream_t stream) {
  (void)in_sizes; (void)n_in; (void)out_size; (void)ws_size;
  fused_mha_fnet<<<256, 768, 0, stream>>>((const char*)d_in[0], (const char*)d_in[1], (const char*)d_in[2], (const char*)d_in[3],
                                          (const char*)d_in[4], (const char*)d_in[5], (const char*)d_in[6], (const char*)d_in[7],
                                          (char*)d_ws, (char*)d_out);
}
```

```cpp
#include <hip/hip_runtime.h>
#include <stdint.h>

typedef __bf16 bf16x8  __attribute__((ext_vector_type(8)));
typedef __bf16 bf16x16 __attribute__((ext_vector_type(16)));
typedef __bf16 bf16x2  __attribute__((ext_vector_type(2)));
typedef short  s16x4   __attribute__((ext_vector_type(4)));
typedef short  s16x8   __attribute__((ext_vector_type(8)));
typedef float  f32x2   __attribute__((ext_vector_type(2)));
typedef float  f32x4   __attribute__((ext_vector_type(4)));
typedef float  f32x8   __attribute__((ext_vector_type(8)));
typedef float  f32x16  __attribute__((ext_vector_type(16)));
typedef unsigned u32x4 __attribute__((ext_vector_type(4)));
typedef unsigned u32x2 __attribute__((ext_vector_type(2)));
typedef unsigned long long u64;
#define LDSP __attribute__((address_space(3)))
#define GLBP __attribute__((address_space(1)))
#define DEV __device__ __forceinline__

constexpr uint32_t WS_Q  = 0;
constexpr uint32_t WS_KV = 2u << 20;
constexpr uint32_t WS_AB = (6u << 20) + 65536;
constexpr uint32_t WS_O  = WS_AB + (4u << 20);
constexpr uint32_t WS_P  = WS_O + (2u << 20);
constexpr uint32_t WS_F  = 24u << 20;
constexpr uint32_t WS_END = 26u << 20;
constexpr uint32_t FS = 256;
constexpr uint32_t F_E = WS_F, F_QKV = WS_F + 0x40000, F_AB = WS_F + 2 * 0x40000, F_O = WS_F + 3 * 0x40000, F_P = WS_F + 4 * 0x40000;
constexpr int SPIN_MAX = 1 << 18;

#define SB __builtin_amdgcn_sched_barrier(0)
#define MFMA(a, b, c) __builtin_amdgcn_mfma_f32_32x32x16_bf16(a, b, c, 0, 0, 0)

DEV void glds(uint32_t ldsaddr, const void* sbase, uint32_t voff) {
  asm volatile("s_mov_b32 m0, %2\n\ts_nop 0\n\tglobal_load_lds_dwordx4 %0, %1" ::"v"(voff), "s"(sbase), "s"(ldsaddr) : "memory");
}
DEV void glds_dev(uint32_t ldsaddr, const void* sbase, uint32_t voff) {
  asm volatile("s_mov_b32 m0, %2\n\ts_nop 0\n\tglobal_load_lds_dwordx4 %0, %1 sc1" ::"v"(voff), "s"(sbase), "s"(ldsaddr) : "memory");
}
constexpr uint32_t WARM = 27136;
DEV void warm(uint32_t ldsaddr, const char* a) {
  asm volatile("s_mov_b32 m0, %1\n\ts_nop 0\n\tglobal_load_lds_dword %0, off" ::"v"(a), "s"(ldsaddr) : "memory");
}
DEV void warm2(uint32_t ldsaddr, const void* sb0, uint32_t vo0, const void* sb1, uint32_t vo1) {
  asm volatile("s_mov_b32 m0, %4\n\ts_mov_b32 exec_lo, 0xffff\n\ts_mov_b32 exec_hi, 0\n\tglobal_load_lds_dword %0, %1\n\t"
               "s_mov_b32 exec_lo, 0xffff0000\n\ts_mov_b32 exec_hi, 0xff\n\tglobal_load_lds_dword %2, %3\n\ts_mov_b64 exec, -1"
               ::"v"(vo0), "s"(sb0), "v"(vo1), "s"(sb1), "s"(ldsaddr) : "memory");
}
template <int N> DEV void wait_vm() { asm volatile("s_waitcnt vmcnt(%0)" ::"n"(N) : "memory"); }
template <int N> DEV void wait_vm_if(int a, int b) {
  asm volatile("s_cmp_lg_u32 %0, %1\n\ts_cbranch_scc1 1f\n\ts_waitcnt vmcnt(%2)\n1:" ::"s"(a), "s"(b), "n"(N) : "scc", "memory");
}
template <int NL, int NS, int MASK> DEV void wait_vm_sel(int g) {
  asm volatile("s_bitcmp1_b32 %0, %1\n\ts_cbranch_scc1 1f\n\ts_waitcnt vmcnt(%3)\n1:\n\ts_waitcnt vmcnt(%2)" ::"n"(MASK), "s"(g), "n"(NL), "n"(NS) : "scc", "memory");
}
DEV void barrier_lgkm() { asm volatile("s_waitcnt lgkmcnt(0)\n\ts_barrier" ::: "memory"); }
DEV bf16x8 ldsr(uint32_t a) { return *(const LDSP bf16x8*)(size_t)a; }
DEV bf16x8 ldstr(uint32_t a0, uint32_t a1) {
  s16x4 x = __builtin_amdgcn_ds_read_tr16_b64_v4i16((LDSP s16x4*)(size_t)a0);
  s16x4 y = __builtin_amdgcn_ds_read_tr16_b64_v4i16((LDSP s16x4*)(size_t)a1);
  return __builtin_bit_cast(bf16x8, (s16x8)__builtin_shufflevector(x, y, 0, 1, 2, 3, 4, 5, 6, 7));
}
DEV unsigned pk(float a, float b) {
  f32x2 v = {a, b};
  return __builtin_bit_cast(unsigned, __builtin_convertvector(v, bf16x2));
}
DEV u32x4 pk8(const f32x16& a, int o, float sc) {
  u32x4 r;
  r[0] = pk(a[o + 0] * sc, a[o + 1] * sc); r[1] = pk(a[o + 2] * sc, a[o + 3] * sc);
  r[2] = pk(a[o + 4] * sc, a[o + 5] * sc); r[3] = pk(a[o + 6] * sc, a[o + 7] * sc);
  return r;
}
DEV u32x4 pk8n(const f32x16& a, int o) {
  u32x4 r;
  r[0] = pk(a[o + 0], a[o + 1]); r[1] = pk(a[o + 2], a[o + 3]);
  r[2] = pk(a[o + 4], a[o + 5]); r[3] = pk(a[o + 6], a[o + 7]);
  return r;
}
DEV u32x4 swap8(u32x4 v) {
  auto a = __builtin_amdgcn_permlane32_swap(v[0], v[2], false, false);
  auto b = __builtin_amdgcn_permlane32_swap(v[1], v[3], false, false);
  u32x4 r; r[0] = a[0]; r[1] = b[0]; r[2] = a[1]; r[3] = b[1];
  return r;
}
DEV f32x16 splat16(float v) {
  f32x16 r;
#pragma unroll
  for (int i = 0; i < 16; ++i) r[i] = v;
  return r;
}

typedef __amdgpu_buffer_rsrc_t rsrc_t;
DEV void st_dev(rsrc_t r, uint32_t off, u32x4 v) { __builtin_amdgcn_raw_buffer_store_b128(v, r, off, 0, 16); }
DEV void st_out(char* out, uint32_t off, u32x4 v) { *(u32x4*)(out + off) = v; }
DEV u32x4 ld_dev(rsrc_t r, uint32_t off) { return __builtin_amdgcn_raw_buffer_load_b128(r, off, 0, 16); }
DEV u64 uni64(u64 v) {
  const unsigned lo = __builtin_amdgcn_readfirstlane((unsigned)v), hi = __builtin_amdgcn_readfirstlane((unsigned)(v >> 32));
  return ((u64)hi << 32) | lo;
}
DEV void publish_nowait(uint32_t cnt, unsigned nwaves, char* ws, uint32_t flag, u64 tag, int l) {
  unsigned old = 0;
  if (l == 0) old = __hip_atomic_fetch_add((LDSP unsigned*)(size_t)cnt, 1u, __ATOMIC_RELAXED, __HIP_MEMORY_SCOPE_WORKGROUP);
  old = __builtin_amdgcn_readfirstlane(old);
  if (old == nwaves - 1 && l == 0) __hip_atomic_store((GLBP u64*)(ws + flag), tag, __ATOMIC_RELAXED, __HIP_MEMORY_SCOPE_AGENT);
}
DEV void poll(const char* ws, uint32_t off, u64 tag) {
#pragma unroll 1
  for (int it = 0; it < SPIN_MAX; ++it) {
    const u64 v = __hip_atomic_load((const GLBP u64*)(ws + off), __ATOMIC_RELAXED, __HIP_MEMORY_SCOPE_AGENT);
    if (__builtin_amdgcn_ballot_w64(v != tag) == 0) break;
    __builtin_amdgcn_s_sleep(1);
  }
  asm volatile("" ::: "memory");
}

constexpr unsigned EMPTY = 0xFFFFFFFFu;
DEV u32x4 not_empty(u32x4 v) {
#pragma unroll
  for (int i = 0; i < 4; ++i) v[i] = v[i] < EMPTY - 1 ? v[i] : EMPTY - 1;
  return v;
}
DEV bool has_empty(u32x4 v) {
  const unsigned a = v[0] > v[1] ? v[0] : v[1], b = v[2] > v[3] ? v[2] : v[3];
  return __builtin_amdgcn_ballot_w64((a > b ? a : b) == EMPTY) != 0;
}
template <int N, class F> DEV int collect(rsrc_t rs, u32x4 (&t)[N], F off) {
  int passes = 0;
  unsigned miss = (1u << N) - 1;
#pragma unroll 1
  for (int it = 0; it < SPIN_MAX; ++it) {
    unsigned still = 0;
#pragma unroll
    for (int i = 0; i < N; ++i) if ((miss >> i & 1) && has_empty(t[i])) still |= 1u << i;
    miss = still;
    if (!miss) break;
    ++passes;
    asm volatile("" ::: "memory");
#pragma unroll
    for (int i = 0; i < N; ++i) if (miss >> i & 1) t[i] = ld_dev(rs, off(i));
  }
  return passes;
}

DEV void lds_arrive(uint32_t cnt, int l) {
  asm volatile("s_waitcnt lgkmcnt(0)" ::: "memory");
  if (l == 0) __hip_atomic_fetch_add((LDSP unsigned*)(size_t)cnt, 1u, __ATOMIC_RELAXED, __HIP_MEMORY_SCOPE_WORKGROUP);
}
template <int SLEEP> DEV void lds_wait(uint32_t cnt, unsigned n) {
#pragma unroll 1
  for (int it = 0; it < SPIN_MAX; ++it) {
    const unsigned v = __hip_atomic_load((LDSP unsigned*)(size_t)cnt, __ATOMIC_RELAXED, __HIP_MEMORY_SCOPE_WORKGROUP);
    if ((unsigned)__builtin_amdgcn_readfirstlane(v) >= n) break;
    if (SLEEP) __builtin_amdgcn_s_sleep(SLEEP);
  }
  asm volatile("" ::: "memory");
}

constexpr uint32_t KV0 = 0, SLOT = 2080, KVBUF = 4 * SLOT;
constexpr uint32_t G0 = 32768, GBUF = 32768;
constexpr uint32_t CNT = 28672;
#define SMF(a, b, c) __builtin_amdgcn_smfmac_f32_32x32x32_bf16(a, b, c, sidx, 0, 0)
#define LO8(x) __builtin_bit_cast(bf16x16, (f32x8)__builtin_shufflevector(x, x, 0, 1, 2, 3, 4, 5, 6, 7))
#define HI8(x) __builtin_bit_cast(bf16x16, (f32x8)__builtin_shufflevector(x, x, 8, 9, 10, 11, 12, 13, 14, 15))
#define PIN(x) do { asm volatile("" ::"v"(x)); SB; } while (0)
#define EXPR(s, a, b) do { _Pragma("unroll") for (int i_ = a; i_ < b; ++i_) s[i_] = __builtin_amdgcn_exp2f(s[i_]); SB; } while (0)

struct AttnState {
  f32x16 sA, sB, oA, oB, nA, nB;
  bf16x8 qA, qB, kf, v0, v1;
};

template <int BUFI, int U>
DEV void attn_step(AttnState& a, uint32_t aK, uint32_t aV0, uint32_t aV1, int sidx) {
  constexpr uint32_t OFF = BUFI * KVBUF + U * SLOT;
  EXPR(a.sA, 0, 2);
  a.oB = SMF(a.v1, HI8(a.sB), a.oB); PIN(a.oB);
  a.v1 = ldsr(aV1 + OFF); SB;
  EXPR(a.sA, 2, 7);
  a.sB = MFMA(a.kf, a.qB, a.nB); PIN(a.sB);
  a.kf = ldsr(aK + OFF); SB;
  EXPR(a.sA, 7, 12);
  a.oA = SMF(a.v0, LO8(a.sA), a.oA); PIN(a.oA);
  EXPR(a.sA, 12, 16);
  EXPR(a.sB, 0, 2);
  a.oA = SMF(a.v1, HI8(a.sA), a.oA); PIN(a.oA);
  EXPR(a.sB, 2, 7);
  a.sA = MFMA(a.kf, a.qA, a.nA); PIN(a.sA);
  EXPR(a.sB, 7, 12);
  a.oB = SMF(a.v0, LO8(a.sB), a.oB); PIN(a.oB);
  a.v0 = ldsr(aV0 + OFF); SB;
  EXPR(a.sB, 12, 16);
}
template <int BUFI>
DEV void attn_stage(AttnState& a, uint32_t aK, uint32_t aV0, uint32_t aV1, int sidx, int stg) {
  barrier_lgkm(); SB;

  attn_step<BUFI, 0>(a, aK, aV0, aV1, sidx);
  attn_step<BUFI, 1>(a, aK, aV0, aV1, sidx);
}

DEV float max16(const f32x16& s) {
  float m = fmaxf(fmaxf(fmaxf(s[0], s[1]), fmaxf(s[2], s[3])), fmaxf(fmaxf(s[4], s[5]), fmaxf(s[6], s[7])));
  float n = fmaxf(fmaxf(fmaxf(s[8], s[9]), fmaxf(s[10], s[11])), fmaxf(fmaxf(s[12], s[13]), fmaxf(s[14], s[15])));
  return fmaxf(m, n);
}
DEV float xhalf_max(float m) {
  auto t = __builtin_amdgcn_permlane32_swap(__float_as_uint(m), __float_as_uint(m), false, false);
  return fmaxf(__uint_as_float(t[0]), __uint_as_float(t[1]));
}
DEV bf16x8 ones8() { u32x4 o = {0x3F803F80u, 0x3F803F80u, 0x3F803F80u, 0x3F803F80u}; return __builtin_bit_cast(bf16x8, o); }
DEV bf16x8 ldf(rsrc_t r, uint32_t off) { return __builtin_bit_cast(bf16x8, ld_dev(r, off)); }

DEV f32x16 attn_exact(rsrc_t rs, uint32_t kvh, bf16x8 q, int l, int sidx) {
  const int h = l >> 5, r32 = l & 31;
  float m = -INFINITY;
  const f32x16 z = splat16(0.f);
#pragma unroll 1
  for (int t = 0; t < 64; ++t) {
    bf16x8 k = ldf(rs, kvh + t * 2048 + l * 16);
    f32x16 s = MFMA(k, q, z);
    m = fmaxf(m, max16(s));
  }
  m = xhalf_max(m);
  const f32x16 nm = splat16(-m);
  f32x16 o = z;
#pragma unroll 1
  for (int t = 0; t < 64; ++t) {
    bf16x8 k = ldf(rs, kvh + t * 2048 + l * 16);
    bf16x8 v0 = ldf(rs, kvh + t * 2048 + 1024 + h * 256 + (r32 & 15) * 16);
    bf16x8 v1 = ldf(rs, kvh + t * 2048 + 1536 + h * 256 + (r32 & 15) * 16);
    if (r32 >= 16) { v0 = ones8(); v1 = ones8(); }
    f32x16 s = MFMA(k, q, nm);
#pragma unroll
    for (int i = 0; i < 16; ++i) s[i] = __builtin_amdgcn_exp2f(s[i]);
    o = SMF(v0, LO8(s), o);
    o = SMF(v1, HI8(s), o);
  }
  return o;
}

constexpr int C0 = 5;
constexpr uint32_t XCODE = 214;
constexpr int XCODEW = 8;
constexpr int GPRIO = 3;
constexpr uint32_t XR = 32768, WR = 98304;
constexpr uint32_t C_KV = CNT, C_AB = CNT + 4, C_G = CNT + 16, C_W = CNT + 20  , C_GD = CNT + 28, C_R = CNT + 32  ,
                   C_PR = CNT + 40  , C_KVS = CNT + 72, C_TP = CNT + 76;
constexpr uint32_t TAGW = CNT + 80;
constexpr uint32_t FSCR = 27392;
DEV uint32_t wslot(int u) { return u % 5 == 4 ? 0u : WR + (uint32_t)(u % 5) * 16384u; }

__global__ __launch_bounds__(768) void fused_mha_fnet(const char* __restrict__ xA, const char* __restrict__ wqkv, const char* __restrict__ wo,
                                                      const char* __restrict__ xB, const char* __restrict__ cre, const char* __restrict__ cim,
                                                      const char* __restrict__ hre, const char* __restrict__ him, char* __restrict__ ws,
                                                      char* __restrict__ out) {
  __shared__ __attribute__((aligned(1024))) char smem[163840];
  const uint32_t lds0 = (uint32_t)(size_t)(LDSP char*)smem;
  const int l = threadIdx.x & 63, w = __builtin_amdgcn_readfirstlane(threadIdx.x >> 6);
  const int h = l >> 5, r32 = l & 31;
  const int b = blockIdx.x, xcd = b & 7, idx = b >> 3;
  const int mt = (xcd >> 1) * 8 + (idx >> 2), nt = (xcd & 1) * 4 + (idx & 3);
  const int head = xcd * 4 + (idx >> 3), qblk = idx & 7;
  const int st = xcd * 4 + (idx >> 3), n3 = idx & 7;

  uint64_t pc;
  asm volatile("s_getpc_b64 %0" : "=s"(pc));
  const uint32_t wvx = (uint32_t)(mt * 64 + (nt & 3) * 16 + l) * 1024u + w * 128u;
  const uint32_t wvw = (uint32_t)(w * 64 + (mt & 7) * 8 + (l & 7)) * 3072u + (uint32_t)(l >> 3) * 1024u + nt * 128u - 2048u;
  auto warmP = [&]() { warm2(lds0 + WARM, xA, wvx, wqkv, wvw); };
  if (w < C0) warmP();
  if (w == XCODEW) {
    const uint32_t ln = (uint32_t)idx * 8u + (uint32_t)l;
    if (l < 8 && ln < XCODE) warm(lds0 + WARM, (const char*)(pc & ~127ull) + ln * 128u);
  }
  SB;
  const rsrc_t rs = __builtin_amdgcn_make_buffer_rsrc(ws, 0, WS_END, 0x00020000);
  const uint32_t xrd = (uint32_t)r32 * 128u + (uint32_t)((h ^ ((r32 >> 1) & 7)) * 16);
  auto issueKV = [&](int g, int p, uint32_t buf) {
    const char* kvb = ws + WS_KV + (size_t)head * 131072 + (g >> 1) * 65536;
    const uint32_t d = lds0 + KV0 + buf + g * SLOT;
    const uint32_t t = 2 * (p & 15) + (g & 1), nx = (t + 1) & 31;
    glds_dev(d, kvb + nx * 2048, l * 16);
    glds_dev(d + 1024, kvb, 1024 + l * 16 + (l < 32 ? nx : t) * 2048);
  };

  if (w < 8) {
    u64 eraw;
    asm volatile("global_load_dwordx2 %0, %1, %2 sc1" : "=&v"(eraw) : "v"(0u), "s"(ws + F_E) : "memory");
    const int wm = w & 1, wn = (w >> 1) & 1;
    *(LDSP unsigned*)(size_t)(lds0 + CNT + l * 4) = 0u;
    const uint32_t tile = mt * 2 + wm, head0 = nt * 4 + wn * 2;
    u64 tag;
    {
      const int q = (l & 15) >> 2, pp = l & 3, blk = (l >> 4) & 1;
      const uint32_t rdX = lds0 + XR + wm * 4096 + xrd;
      const uint32_t rdW = lds0 + (w < 4 ? 0u : 8192u) + (uint32_t)(8 * h + q) * 128u +
                           (uint32_t)(((4 * wn + 2 * blk + (pp >> 1)) ^ (4 * (q >> 1))) * 16 + 8 * (pp & 1));
      uint32_t rslot = WR;
      auto nextslot = [&]() { rslot = rslot == 0 ? WR : (rslot == WR + 49152 ? 0u : rslot + 16384); };
      struct Fr { bf16x8 x[4], w[4]; };
      Fr fa, fb;
      auto rd = [&](Fr& f, uint32_t xoff) {
        const uint32_t bx = rdX + xoff, bw = rdW + rslot;
#pragma unroll
        for (int ks = 0; ks < 4; ++ks) { f.x[ks] = ldsr(bx ^ (ks << 5)); f.w[ks] = ldstr(bw + ks * 2048, bw + ks * 2048 + 512); }
        nextslot();
        SB;
      };
      f32x16 a0 = splat16(0.f), a1 = a0;
      auto mmWX = [&](const Fr& f) { a0 = MFMA(f.w[0], f.x[0], a0); a1 = MFMA(f.w[1], f.x[1], a1); a0 = MFMA(f.w[2], f.x[2], a0); a1 = MFMA(f.w[3], f.x[3], a1); SB; };
      auto mmXW = [&](const Fr& f) { a0 = MFMA(f.x[0], f.w[0], a0); a1 = MFMA(f.x[1], f.w[1], a1); a0 = MFMA(f.x[2], f.w[2], a0); a1 = MFMA(f.x[3], f.w[3], a1); SB; };
      const int kq2 = w >> 2;
      auto gate = [&](int c) { wait_vm_if<0>(w, c); };
      static_assert(C0 >= 2, "chunks 0 and 1 are requested at the start");
      wait_vm_if<1>(w >> 1, 0);
      asm volatile("s_barrier" ::: "memory");
      if (w >= C0) warmP();
      gate(2);
      barrier_lgkm();
      rd(fa, 0);
      const uint32_t qslot = WS_Q + ((head0 + kq2) * 64 + tile) * 1024 + l * 16;
      auto clearq = [&]() { st_dev(rs, qslot, (u32x4){EMPTY, EMPTY, EMPTY, EMPTY}); };
      if (w < 4) {
#pragma unroll 1
        for (int p = 1; p < 9; p += 2) {
          gate(p + 2); barrier_lgkm(); rd(fb, p * 8192); mmWX(fa);
          gate(p + 3); barrier_lgkm(); rd(fa, p < 7 ? (p + 1) * 8192 : kq2 * 8192); mmWX(fb);
          if (p == 5) clearq();
        }

#pragma unroll
        for (int i = 0; i < 16; ++i) a0[i] += a1[i];
        const int pr = (r32 & 0x13) | ((r32 & 4) << 1) | ((r32 & 8) >> 1);
#pragma unroll
        for (int hh = 0; hh < 2; ++hh) st_dev(rs, WS_KV + ((head0 + hh) * 64 + tile) * 2048 + (h * 32 + pr) * 16, pk8n(a0, 8 * hh));
      } else {
#pragma unroll 1
        for (int p = 1; p < 9; p += 2) {
          gate(p + 2); barrier_lgkm(); rd(fb, p * 8192); mmXW(fa);
          gate(p + 3); barrier_lgkm(); rd(fa, p < 7 ? (p + 1) * 8192 : kq2 * 8192); mmXW(fb);
          if (p == 5) clearq();
        }

#pragma unroll
        for (int i = 0; i < 16; ++i) a0[i] += a1[i];
        const uint32_t vb = WS_KV + ((head0 + (r32 >> 4)) * 64 + tile) * 2048 + 1024 + h * 256 + (r32 & 15) * 16;
        st_dev(rs, vb, pk8n(a0, 0));
        st_dev(rs, vb + 512, pk8n(a0, 8));
      }
      a0 = splat16(0.f); a1 = a0;
      barrier_lgkm(); rd(fb, (2 + kq2) * 8192); mmWX(fa);
      barrier_lgkm(); rd(fa, (4 + kq2) * 8192); mmWX(fb);
      barrier_lgkm(); rd(fb, (6 + kq2) * 8192);
      mmWX(fa);

      mmWX(fb);
      {
        f32x4 s0, s1;
#pragma unroll
        for (int i = 0; i < 16; ++i) a0[i] += a1[i];
#pragma unroll
        for (int i = 0; i < 4; ++i) { s0[i] = kq2 ? a0[i] : a0[8 + i]; s1[i] = kq2 ? a0[4 + i] : a0[12 + i]; }
        *(LDSP f32x4*)(size_t)(lds0 + w * 2048 + l * 16) = s0;
        *(LDSP f32x4*)(size_t)(lds0 + w * 2048 + 1024 + l * 16) = s1;
        barrier_lgkm();

        const f32x4 r0 = *(const LDSP f32x4*)(size_t)(lds0 + (w ^ 4) * 2048 + l * 16);
        const f32x4 r1 = *(const LDSP f32x4*)(size_t)(lds0 + (w ^ 4) * 2048 + 1024 + l * 16);
        constexpr float QS = 0.25f * 1.4426950408889634f;
        float qv[8];
#pragma unroll
        for (int i = 0; i < 4; ++i) { qv[i] = ((kq2 ? a0[8 + i] : a0[i]) + r0[i]) * QS; qv[4 + i] = ((kq2 ? a0[12 + i] : a0[4 + i]) + r1[i]) * QS; }
        const u32x4 pq = {pk(qv[0], qv[1]), pk(qv[2], qv[3]), pk(qv[4], qv[5]), pk(qv[6], qv[7])};
        st_dev(rs, qslot, not_empty(pq));
        barrier_lgkm();
      }

    }
    wait_vm<1>();

    asm volatile("" : "+v"(eraw));
    tag = uni64(eraw) + 1;
    publish_nowait(lds0 + C_KV, 8, ws, F_QKV + (nt * 32 + mt) * FS, tag, l);

    const int qg = w & 3, kh = w >> 2;
    const uint32_t kvh = WS_KV + head * 131072;
    if (kh == 1) {
      poll(ws, F_QKV + (xcd * 32 + r32) * FS, tag);
      if (w == 4 && l == 12) *(LDSP u64*)(size_t)(lds0 + TAGW) = tag;
      if (w == 4 && l < 12) { u32x4 o = {0x3F803F80u, 0x3F803F80u, 0x3F803F80u, 0x3F803F80u}; *(LDSP u32x4*)(size_t)(lds0 + KV0 + l * SLOT + 2048) = o; }
      if (qg & 1) issueKV(qg, -1, 2 * KVBUF);
      issueKV(qg, 0, 0);
      lds_arrive(lds0 + C_KVS, l);
    } else {
      lds_wait<1>(lds0 + C_KVS, 4);
    }

    int sidx = 0xDDDD;
    asm volatile("" : "+v"(sidx));
    AttnState a;
    {
      u32x4 qt[2];
      auto qoff = [&](int i) { return WS_Q + (head * 64 + qblk * 8 + qg * 2 + i) * 1024 + l * 16; };
      qt[0] = ld_dev(rs, qoff(0)); qt[1] = ld_dev(rs, qoff(1));
      collect<2>(rs, qt, qoff);
      a.qA = __builtin_bit_cast(bf16x8, qt[0]);
      a.qB = __builtin_bit_cast(bf16x8, qt[1]);
    }
    const uint32_t sb = lds0 + KV0 + kh * 2 * SLOT;
    const bool onesl = (r32 >= 16 && r32 < 20) || r32 >= 28;
    const uint32_t vrow = h * 256 + (r32 < 16 ? r32 * 16 : 0);
    const uint32_t aK = sb + l * 16;
    const uint32_t aV0 = sb + (onesl ? 2048u : 1024u + vrow);
    const uint32_t aV1 = sb + (onesl ? 2048u : 1536u + vrow);
    if (kh == 1) { wait_vm<0>(); lds_arrive(lds0 + C_TP, l); }
    lds_wait<1>(lds0 + C_TP, 4);

    {
      a.kf = ldsr(aK + 2 * KVBUF + SLOT);
      const bf16x8 k0 = ldsr(lds0 + KV0 + 2 * KVBUF + SLOT + l * 16);
      a.v0 = ldsr(aV0 + 2 * KVBUF + SLOT);
      const f32x16 z = splat16(0.f);
      f32x16 t0 = MFMA(k0, a.qA, z), t1 = MFMA(k0, a.qB, z);
      a.nA = splat16(-(xhalf_max(max16(t0)) + 64.f));
      a.nB = splat16(-(xhalf_max(max16(t1)) + 64.f));
      a.oA = z; a.oB = z; a.sB = z;
      a.v1 = __builtin_bit_cast(bf16x8, (u32x4){0u, 0u, 0u, 0u});
      a.sA = MFMA(a.kf, a.qA, a.nA);
    }

#pragma unroll 1
    for (int it = 0; it < 5; ++it) {
      attn_stage<0>(a, aK, aV0, aV1, sidx, 3 * it);
      attn_stage<1>(a, aK, aV0, aV1, sidx, 3 * it + 1);
      attn_stage<2>(a, aK, aV0, aV1, sidx, 3 * it + 2);
    }
    attn_stage<0>(a, aK, aV0, aV1, sidx, 15);
    a.oB = SMF(a.v1, HI8(a.sB), a.oB);

    float o[8], lsum;
    {
      const float lA = h ? a.oA[12] : a.oA[8], lB = h ? a.oB[12] : a.oB[8];
      const uint32_t cbs = lds0 + KVBUF + (qg * 2 + kh) * 2304, cbr = lds0 + KVBUF + (qg * 2 + (kh ^ 1)) * 2304;
      f32x4 s0, s1;
#pragma unroll
      for (int i = 0; i < 4; ++i) { s0[i] = kh ? a.oA[i] : a.oB[i]; s1[i] = kh ? a.oA[4 + i] : a.oB[4 + i]; }
      *(LDSP f32x4*)(size_t)(cbs + l * 16) = s0;
      *(LDSP f32x4*)(size_t)(cbs + 1024 + l * 16) = s1;
      *(LDSP float*)(size_t)(cbs + 2048 + l * 4) = kh ? lA : lB;

      lds_arrive(lds0 + C_PR + qg * 4, l);

      lds_wait<1>(lds0 + C_PR + qg * 4, 2);

      const f32x4 r0 = *(const LDSP f32x4*)(size_t)(cbr + l * 16), r1 = *(const LDSP f32x4*)(size_t)(cbr + 1024 + l * 16);
      lsum = (kh ? lB : lA) + *(const LDSP float*)(size_t)(cbr + 2048 + l * 4);
#pragma unroll
      for (int i = 0; i < 4; ++i) { o[i] = (kh ? a.oB[i] : a.oA[i]) + r0[i]; o[4 + i] = (kh ? a.oB[4 + i] : a.oA[4 + i]) + r1[i]; }
    }
    {
      float mx = 0.f;
#pragma unroll
      for (int i = 0; i < 8; ++i) mx = fmaxf(mx, fabsf(o[i]));
      const bool ok = lsum >= 0x1p-100f && lsum <= 0x1p120f && mx <= 0x1p126f && (o[0] + o[1] + o[2] + o[3] + o[4] + o[5] + o[6] + o[7]) * 0.f == 0.f;

      if (__builtin_amdgcn_ballot_w64(!ok) != 0) {
        const f32x16 e = attn_exact(rs, kvh, kh ? a.qB : a.qA, l, sidx);
#pragma unroll
        for (int i = 0; i < 8; ++i) o[i] = e[i];
        lsum = h ? e[12] : e[8];
      }

      const float r = __builtin_amdgcn_rcpf(lsum);
      u32x4 pa;
#pragma unroll
      for (int i = 0; i < 4; ++i) pa[i] = pk(o[2 * i] * r, o[2 * i + 1] * r);
      st_dev(rs, WS_O + ((qblk * 8 + qg * 2 + kh) * 32 + head) * 1024 + l * 16, not_empty(pa));
    }

    const int w0 = w & 1, kq = w >> 1;
    bf16x8 wf0[8], wf1[8];
    u32x4 of[8];
    auto ooff = [&](int i) { return WS_O + ((st * 2 + w0) * 32 + 8 * kq + ((i + n3) & 7)) * 1024 + l * 16; };
    {
      const int q4 = (l & 15) >> 2, p4 = l & 3, blk = (l >> 4) & 1;
      const uint32_t rdW = lds0 + G0 + kq * 16384 + (uint32_t)(4 * h + q4) * 128u +
                           (uint32_t)(((2 * blk + (p4 >> 1)) ^ (4 * (q4 >> 1))) * 16 + 8 * (p4 & 1));
      lds_wait<1>(lds0 + C_W, 4);
#pragma unroll
      for (int i = 0; i < 8; ++i) of[i] = ld_dev(rs, ooff(i));
      SB;
      lds_wait<1>(lds0 + C_W + (kq >> 1) * 4, 4);
#pragma unroll
      for (int i = 0; i < 8; ++i) {
        const uint32_t ro = ((i + n3) & 7) * 2048;
        wf0[i] = ldstr(rdW + ro, rdW + ro + 1024);
        wf1[i] = ldstr((rdW ^ 64) + ro, (rdW ^ 64) + ro + 1024);
      }
    }

    collect<8>(rs, of, ooff);

    f32x16 c0 = splat16(0.f), c1 = c0;
#pragma unroll
    for (int i = 0; i < 8; ++i) { const bf16x8 o8 = __builtin_bit_cast(bf16x8, of[i]); c0 = MFMA(wf0[i], o8, c0); c1 = MFMA(wf1[i], o8, c1); }

    {
      const uint32_t red = lds0 + G0 + 2 * GBUF + w0 * 32768 + l * 16;
      auto put = [&](const f32x16& c, int oo, int d) {
        f32x4 u0, u1;
#pragma unroll
        for (int i = 0; i < 4; ++i) { u0[i] = c[oo + i]; u1[i] = c[oo + 4 + i]; }
        *(LDSP f32x4*)(size_t)(red + (d * 4 + kq) * 2048) = u0;
        *(LDSP f32x4*)(size_t)(red + (d * 4 + kq) * 2048 + 1024) = u1;
      };
      lds_wait<1>(lds0 + C_GD, 4);
      put(c0, 0, 0); put(c0, 8, 1); put(c1, 0, 2); put(c1, 8, 3);
      lds_arrive(lds0 + C_R + w0 * 4, l);
      lds_wait<0>(lds0 + C_R + w0 * 4, 4);
      f32x4 s0 = {0.f, 0.f, 0.f, 0.f}, s1 = s0;
#pragma unroll
      for (int s = 0; s < 4; ++s) {
        s0 += *(const LDSP f32x4*)(size_t)(red + (kq * 4 + s) * 2048);
        s1 += *(const LDSP f32x4*)(size_t)(red + (kq * 4 + s) * 2048 + 1024);
      }
      u32x4 pv = {pk(s0[0], s0[1]), pk(s0[2], s0[3]), pk(s1[0], s1[1]), pk(s1[2], s1[3])};
      st_out(out, (uint32_t)(st * 64 + w0 * 32 + r32) * 2048u + (n3 * 64 + kq * 16 + 8 * h) * 2, swap8(pv));
    }
    if (b == 0 && threadIdx.x == 0) __hip_atomic_store((GLBP u64*)(ws + F_E), tag, __ATOMIC_RELAXED, __HIP_MEMORY_SCOPE_AGENT);

  } else {
    const int g = w - 8;
    __builtin_amdgcn_s_setprio(GPRIO);

    const char* hw = nt < 4 ? hre : him;
    const uint32_t vo0 = (uint32_t)(l >> 3) * 1024u + (uint32_t)(((l & 7) ^ (l >> 4)) * 16);
    const uint32_t vo1 = (uint32_t)(8 + (l >> 3)) * 1024u + (uint32_t)(((l & 7) ^ (4 + (l >> 4))) * 16);
    const char* hx = xB + (size_t)(mt * 64 + 16 * g) * 1024;
    const char* hwg = hw + (size_t)((nt & 3) * 128 + 32 * g) * 1024;
    auto issueHX = [&](int c) {
      glds(lds0 + XR + c * 8192 + g * 2048, hx + c * 128, vo0);
      glds(lds0 + XR + c * 8192 + g * 2048 + 1024, hx + c * 128, vo1);
    };
    auto issueHW = [&](int c) {
      const uint32_t d = lds0 + WR + ((c + 2) & 3) * 16384 + g * 4096;
      glds(d, hwg + c * 128, vo0);
      glds(d + 1024, hwg + c * 128, vo1);
      glds(d + 2048, hwg + 16384 + c * 128, vo0);
      glds(d + 3072, hwg + 16384 + c * 128, vo1);
    };
    {
      const char* gx = xA + (size_t)(mt * 64 + 16 * g) * 1024;
      const char* gw = wqkv + (size_t)(16 * g) * 3072 + nt * 128;
      const uint32_t vw = (uint32_t)(l >> 3) * 3072u + (uint32_t)(((l & 7) ^ (((l >> 4) & 1) << 2)) * 16);
      auto issue = [&](int u) {
        const uint32_t bb = lds0 + wslot(u) + g * 2048;
        if (u < 8) {
          glds(lds0 + XR + u * 8192 + g * 2048, gx + u * 128, vo0);
          glds(lds0 + XR + u * 8192 + g * 2048 + 1024, gx + u * 128, vo1);
        }
#pragma unroll
        for (int c = 0; c < 2; ++c) {
          const char* src = u < 8 ? gw + (size_t)u * (64 * 3072) + (c + 1) * 1024 : gw + (size_t)(2 * (u - 8) + c) * (64 * 3072);
          glds(bb + c * 8192, src, vw);
          glds(bb + c * 8192 + 1024, src + 8 * 3072, vw);
        }
      };
      asm volatile("s_barrier" ::: "memory");

      issue(0); issue(1);

      wait_vm<6>();

      auto bar = [&]() { asm volatile("s_barrier" ::: "memory"); };
      bar(); issue(2);
      wait_vm<6>(); bar(); issue(3);
      wait_vm<6>(); bar(); issue(4);
      auto warmH = [&](int gg) {
        const char* px = xB + (size_t)(mt * 64 + (nt & 3) * 16 + (l & 15)) * 1024;
        const char* pw = hw + (size_t)((nt & 3) * 128 + (mt & 7) * 16 + (l & 15)) * 1024;
        if (g == gg) warm(lds0 + WARM, (l < 32 ? px : pw) + g * 256 + ((l >> 4) & 1) * 128);
      };
      wait_vm<6>(); bar(); issue(5);
      wait_vm<6>();
      bar(); issue(6); warmH(0);
      wait_vm_sel<7, 6, 1>(g);
      bar(); issue(7); issue(8); warmH(1);
      wait_vm_sel<11, 10, 3>(g);
      bar(); issue(9); issue(10); warmH(2);
      wait_vm_sel<13, 12, 6>(g);
      bar(); issue(11); warmH(3);
      wait_vm_sel<13, 12, 14>(g);
      bar();
      wait_vm_sel<9, 8, 12>(g);
      bar(); issueHW(0); issueHX(0); issueHX(1);
      wait_vm_sel<13, 12, 12>(g);
      bar(); issueHW(1); issueHX(2); issueHX(3);
      wait_vm_sel<17, 16, 8>(g);
      bar(); issueHW(2); issueHX(4); issueHX(5);
    }

    f32x16 c00 = splat16(0.f), c01 = c00, c10 = c00, c11 = c00;
    const uint32_t hrX = lds0 + XR + xrd, hrE = lds0 + WR + g * 4096 + xrd;
    struct HF { bf16x8 x0[4], x1[4], e[4]; };
    HF ha, hb;
    auto rdH = [&](HF& f, int c) {
#pragma unroll
      for (int ks = 0; ks < 4; ++ks) {
        f.x0[ks] = ldsr((hrX + c * 8192) ^ (ks << 5)); f.x1[ks] = ldsr(((hrX + c * 8192) ^ (ks << 5)) + 4096);
        f.e[ks] = ldsr((hrE + ((c + 2) & 3) * 16384) ^ (ks << 5));
      }
      SB;
    };
    auto mmH = [&](const HF& f) {
#pragma unroll
      for (int ks = 0; ks < 4; ++ks) { c00 = MFMA(f.x0[ks], f.e[ks], c00); c01 = MFMA(f.x1[ks], f.e[ks], c01); }
      SB;
    };
    auto lgkm12 = [&]() { asm volatile("s_waitcnt lgkmcnt(12)" ::: "memory"); };
    auto lgkm0 = [&]() { asm volatile("s_waitcnt lgkmcnt(0)" ::: "memory"); };
    unsigned gs = 0;
    auto gsync = [&]() { gs += 4; lds_arrive(lds0 + C_G, l); lds_wait<0>(lds0 + C_G, gs); };
    barrier_lgkm();
    asm volatile("s_barrier" ::: "memory");
    issueHW(3); issueHX(6); issueHX(7);
    wait_vm<8>();
    gsync();

    rdH(ha, 0); rdH(hb, 1);
    lgkm12(); issueHW(4); mmH(ha);
    rdH(ha, 2);
    lgkm12(); issueHW(5); mmH(hb);
    wait_vm<8>();
    gsync();
    rdH(hb, 3);
    issueHW(6); mmH(ha);
    wait_vm<8>(); rdH(ha, 4);
    lgkm12(); issueHW(7); mmH(hb);

    wait_vm<8>(); rdH(hb, 5);
    mmH(ha);
    wait_vm<4>(); rdH(ha, 6);
    mmH(hb);
    wait_vm<0>(); rdH(hb, 7);
    mmH(ha); mmH(hb);

    {
      const float sg = nt < 4 ? 1.f : -1.f;
      const uint32_t ab = WS_AB + nt * (512u << 10) + (mt * 4) * 4096 + g * 1024 + l * 16;
      st_dev(rs, ab, swap8(pk8(c00, 0, sg)));
      st_dev(rs, ab + 4096, swap8(pk8(c00, 8, sg)));
      st_dev(rs, ab + 8192, swap8(pk8(c01, 0, sg)));
      st_dev(rs, ab + 12288, swap8(pk8(c01, 8, sg)));
    }
    gsync();

    const int ge = g & 1, gt = g >> 1;
    const int q = xcd >> 1, mtile = (xcd & 1) * 8 + (idx >> 2), ntile = idx & 3;
    const char* gbase; uint32_t pstride, qstride, go0, go1;
    if (g < 2) {
      gbase = (q < 2 ? cre : cim) + (size_t)(mtile * 128 + g * 64) * 4096 + (q & 1) * 2048;
      pstride = 128; qstride = 65536;
      go0 = (uint32_t)(l >> 3) * 4096u + (uint32_t)(((l & 7) ^ (l >> 4)) * 16);
      go1 = (uint32_t)(8 + (l >> 3)) * 4096u + (uint32_t)(((l & 7) ^ (4 + (l >> 4))) * 16);
    } else {
      gbase = ws + WS_AB + (size_t)((q >> 1) * 4 + ntile) * (512u << 10) + (size_t)(q & 1) * 64 * 4096 + (g - 2) * 8192;
      pstride = 16384; qstride = 2048;
      go0 = l * 16; go1 = l * 16 + 1024;
    }
    const uint32_t pg = WS_P + (((mtile * 4 + ntile) * 4 + g) * 16) * 2048 + l * 16;
    auto issueG2 = [&](int u, int i0) {
      const char* sbp = gbase + (size_t)u * pstride + (size_t)(i0 >> 1) * qstride;
      const uint32_t d = lds0 + G0 + (u & 3) * GBUF + g * 8192 + i0 * 1024;
      glds_dev(d, sbp, go0);
      glds_dev(d + 1024, sbp, go1);
    };
    auto issueG = [&](int u) { issueG2(u, 0); issueG2(u, 2); issueG2(u, 4); issueG2(u, 6); };
    lds_wait<1>(lds0 + C_KVS, 4);
    const u64 tag = uni64(*(const LDSP u64*)(size_t)(lds0 + TAGW));
    issueKV(g, 1, KVBUF);
    wait_vm<2>();
    publish_nowait(lds0 + C_AB, 4, ws, F_AB + (nt * 32 + mt) * FS, tag, l);

    barrier_lgkm();

    issueKV(g, 2, 2 * KVBUF);
    if (g < 2) { issueG(0); issueG(1); issueG(2); wait_vm<26>(); } else { wait_vm<2>(); }
    barrier_lgkm();
    issueKV(g, 3, 0);
    warm(lds0 + WARM, wo + (size_t)((idx >> 3) * 128 + g * 32 + r32) * 1024 + (idx & 7) * 128);
    if (g >= 2) {
      poll(ws, F_AB + (((q >> 1) * 4 + ntile) * 32 + (q & 1) * 16 + (l & 15)) * FS, tag);
      issueG(0); issueG(1); issueG(2);
    } else {
      wait_vm<27>();
    }

    barrier_lgkm();
    issueKV(g, 4, KVBUF);
    wait_vm_sel<18, 2, 12>(g);

    const uint32_t rdC = lds0 + G0 + (uint32_t)(64 * gt) * 128u + xrd;
    const uint32_t rdA = lds0 + G0 + 16384 + ge * 2048 + l * 16;
    c00 = splat16(0.f); c01 = c00; c10 = c00; c11 = c00;
    auto unit = [&](int u, auto dma) {
      const uint32_t so = (u & 3) * GBUF;
      const uint32_t ra = rdA + so, rc = rdC + so;
      bf16x8 e0 = ldsr(ra), e1 = ldsr(ra + 1024), t0 = ldsr(rc), t1 = ldsr(rc + 4096);
      dma(4);
#pragma unroll
      for (int ks = 0; ks < 4; ++ks) {
        bf16x8 f0, f1, u0, u1;
        if (ks < 3) {
          f0 = ldsr(ra + (ks + 1) * 4096); f1 = ldsr(ra + (ks + 1) * 4096 + 1024);
          u0 = ldsr(rc ^ ((ks + 1) << 5)); u1 = ldsr((rc ^ ((ks + 1) << 5)) + 4096);
        }
        SB;
        __builtin_amdgcn_s_setprio(0);
        c00 = MFMA(e0, t0, c00); c01 = MFMA(e0, t1, c01);
        c10 = MFMA(e1, t0, c10); c11 = MFMA(e1, t1, c11);
        __builtin_amdgcn_s_setprio(GPRIO);
        SB;
        dma(ks);
        if (ks < 3) { e0 = f0; e1 = f1; t0 = u0; t1 = u1; }
      }
    };
    uint32_t kvbuf = 2 * KVBUF;
    const uint32_t fchk = l < 32 ? F_O + ((st >> 2) * 32 + l) * FS : F_P + ((mtile * 4 + ntile) * 4 + (l & 3)) * FS;
#pragma unroll 1
    for (int p = 3; p < 16; ++p) {
      barrier_lgkm();

      unit(p - 3, [&](int k) { if (k == 4) { if (p <= 13) issueKV(g, p + 2, kvbuf); } else issueG2(p, 2 * k); });
      kvbuf = kvbuf == 2 * KVBUF ? 0 : kvbuf + KVBUF;
      if (__builtin_expect((0x2278 >> p) & 1, 0)) {
        if (p < 7) {
          const u32x4 e = {EMPTY, EMPTY, EMPTY, EMPTY};
          const uint32_t o = p == 3 ? WS_O + ((qblk * 8 + 2 * g) * 32 + head) * 1024 + l * 16 : pg + (((q + p - 3) & 3) * 4 + q) * 2048;
          st_dev(rs, o, e); st_dev(rs, o + (p == 3 ? 32768 : 1024), e);
        } else if (p == 9) {
          if (g == 0 && l < 2)
            __hip_atomic_store((GLBP u64*)(ws + (l ? F_P + ((mtile * 4 + ntile) * 4 + q) * FS : F_O + (qblk * 32 + head) * FS)), tag, __ATOMIC_RELAXED, __HIP_MEMORY_SCOPE_AGENT);
        } else if (g == 3) {
          glds_dev(lds0 + FSCR, ws, fchk);
        }
      }
      if (p == 3) wait_vm<10>();
      else if (p <= 13) wait_vm<18>();
      else wait_vm<16>();
    }
    const uint32_t voW = (uint32_t)(l >> 3) * 1024u + (uint32_t)(((l & 7) ^ (((l >> 4) & 1) << 2)) * 16);
    auto issueW2 = [&](int hf, int k) {
      const int row = hf * 256 + g * 64 + k * 16;
      glds(lds0 + G0 + row * 128, wo + (size_t)row * 1024 + n3 * 128, voW);
      glds(lds0 + G0 + row * 128 + 1024, wo + (size_t)(row + 8) * 1024 + n3 * 128, voW);
    };
    if (g == 3 && __builtin_amdgcn_ballot_w64(*(const LDSP u64*)(size_t)(lds0 + FSCR + l * 16) != tag) != 0) poll(ws, fchk, tag);
    gsync();

    unit(13, [&](int k) { if (k < 4) issueW2(0, k); });
    wait_vm<16>();
    gsync();
    unit(14, [&](int k) { if (k < 4) issueW2(1, k); });
    wait_vm<8>();
    lds_arrive(lds0 + C_W, l);
    gsync();
    unit(15, [&](int) {});
    lds_arrive(lds0 + C_GD, l);

    f32x16 own;
    auto send = [&](const f32x16& c, int dq) {
      st_dev(rs, pg + (dq * 4 + q) * 2048, not_empty(pk8n(c, 0)));
      st_dev(rs, pg + (dq * 4 + q) * 2048 + 1024, not_empty(pk8n(c, 8)));
    };
    if (q != 0) send(c00, 0);
    if (q != 1) send(c01, 1);
    if (q != 2) send(c10, 2);
    if (q != 3) send(c11, 3);
    if (q == 0) own = c00; else if (q == 1) own = c01; else if (q == 2) own = c10; else own = c11;
    wait_vm<6>();
    lds_arrive(lds0 + C_W + 4, l);

    {
      u32x4 pv[6];
      auto poff = [&](int i) { return pg + (q * 4 + ((q + 1 + (i >> 1)) & 3)) * 2048 + (i & 1) * 1024; };
#pragma unroll
      for (int i = 0; i < 6; ++i) pv[i] = ld_dev(rs, poff(i));
      collect<6>(rs, pv, poff);

      const uint32_t orow = (uint32_t)(mtile * 128 + gt * 64 + (q & 1) * 32 + r32) * 2048u + 1024 + (ntile * 128 + ge * 64 + (q >> 1) * 32 + 8 * h) * 2;
#pragma unroll
      for (int r = 0; r < 2; ++r) {
        u32x4 o;
#pragma unroll
        for (int i = 0; i < 4; ++i) {
          float lo = own[8 * r + 2 * i], hi = own[8 * r + 2 * i + 1];
#pragma unroll
          for (int s = 0; s < 3; ++s) { lo += __uint_as_float(pv[2 * s + r][i] << 16); hi += __uint_as_float(pv[2 * s + r][i] & 0xffff0000u); }
          o[i] = pk(lo, hi);
        }
        st_out(out, orow + 32 * r, swap8(o));
      }
    }

  }
}

extern "C" void kernel_launch(void* const* d_in, const int* in_sizes, int n_in, void* d_out, int out_size, void* d_ws, size_t ws_size,
                              hipStream_t stream) {
  (void)in_sizes; (void)n_in; (void)out_size; (void)ws_size;
  fused_mha_fnet<<<256, 768, 0, stream>>>((const char*)d_in[0], (const char*)d_in[1], (const char*)d_in[2], (const char*)d_in[3],
                                          (const char*)d_in[4], (const char*)d_in[5], (const char*)d_in[6], (const char*)d_in[7],
                                          (char*)d_ws, (char*)d_out);
}
```
